```python
import torch

def _softmax(x, axis=-1):
    return torch.softmax(x, dim=axis)
def _gelu(x):
    return torch.nn.functional.gelu(x, approximate="tanh")
def _silu(x):
    return torch.nn.functional.silu(x)
def _sigmoid(x):
    return torch.sigmoid(x)
def _layernorm(x, axis=-1, eps=1e-5):
    m = x.mean(dim=axis, keepdim=True)
    v = x.var(dim=axis, keepdim=True, unbiased=False)
    return (x - m) / torch.sqrt(v + eps)
def _scatter_add(tgt, idx, src, axis=0):
    return tgt.clone().index_add_(axis, idx, src)
def _segment_sum(src, seg, n):
    out = torch.zeros((n,) + src.shape[1:], dtype=src.dtype)
    return out.index_add_(0, seg, src)
def _pad(x, pw):
    flat = tuple(v for pair in reversed(pw) for v in pair)
    return torch.nn.functional.pad(x, flat)

kernel_name = 'parallel_sum__sliding_block__dense_activation_gelu__prompt'

def setup_inputs():

    _w = (lambda a: (lambda t: t.to(torch.bfloat16) if t.is_floating_point() else t)(torch.as_tensor(a)))
    def _A_setup():
        B, S, D = 1, 2048, 1024
        return {
            "x": _w((lambda *s: torch.randn(*s, dtype=torch.bfloat16) * 0.05)(B, S, D)),
            "wqkv": _w((lambda *s: torch.randn(*s, dtype=torch.bfloat16) * 0.05)(D, 3 * D)),
            "wo": _w((lambda *s: torch.randn(*s, dtype=torch.bfloat16) * 0.05)(D, D)),
        }
    _A_in = _A_setup()

    def _B_setup():
        B, S, D, F = 1, 2048, 1024, 3584
        return {
            "x": _w((lambda *s: torch.randn(*s, dtype=torch.bfloat16) * 0.05)(B, S, D)),
            "w1": _w((lambda *s: torch.randn(*s, dtype=torch.bfloat16) * 3.2)(D, F) * (D ** -0.5)),
            "b1": _w((lambda *s: torch.randn(*s, dtype=torch.bfloat16) * 3.2)(F) * 0.01),
            "w2": _w((lambda *s: torch.randn(*s, dtype=torch.bfloat16) * 0.4)(F, D) * (F ** -0.5)),
            "b2": _w((lambda *s: torch.randn(*s, dtype=torch.bfloat16) * 0.4)(D) * 0.01),
        }
    _B_in = _B_setup()

    return {
        "A_x": _A_in["x"],
        "A_wqkv": _A_in["wqkv"],
        "A_wo": _A_in["wo"],
        "B_x": _B_in["x"],
        "B_w1": _B_in["w1"],
        "B_b1": _B_in["b1"],
        "B_w2": _B_in["w2"],
        "B_b2": _B_in["b2"],
    }

def reference(A_x, A_wqkv, A_wo, B_x, B_w1, B_b1, B_w2, B_b2):
    def _A_ref(x, wqkv, wo):
        B, S, D = x.shape
        H = 32; Dh = D // H; W = 256
        qkv = (lambda x, *d: x.permute(*d))((x @ wqkv).reshape(B, S, 3, H, Dh), 2, 0, 3, 1, 4)
        q, k, v = qkv[0], qkv[1], qkv[2]
        scores = torch.einsum("bhqd,bhkd->bhqk", q, k) * (Dh ** -0.5)
        idx = torch.arange(S)
        mask = (idx[:, None] - idx[None, :] >= 0) & (idx[:, None] - idx[None, :] < W)
        scores = torch.where(mask[None, None], scores, -1e9)
        p = _softmax(scores, axis=-1)
        o = torch.einsum("bhqk,bhkd->bhqd", p, v)
        return (lambda x, *d: x.permute(*d))(o, 0, 2, 1, 3).reshape(B, S, D) @ wo

    def _B_ref(x, w1, b1, w2, b2):
        h = torch.einsum("bsd,df->bsf", x, w1) + b1
        h = _gelu(h)
        return torch.einsum("bsf,fd->bsd", h, w2) + b2

    x = A_x
    xn = _layernorm(x, axis=-1)
    return x + _A_ref(xn, A_wqkv, A_wo) + _B_ref(xn, B_w1, B_b1, B_w2, B_b2)
```

```cpp
#include <hip/hip_runtime.h>
#include <hip/hip_bf16.h>
typedef unsigned short u16;
__device__ inline float b2f(u16 v){ return __uint_as_float(((unsigned)v)<<16); }
__device__ inline u16 f2b(float f){ unsigned u=__float_as_uint(f); u += 0x7fffu + ((u>>16)&1u); return (u16)(u>>16); }
__device__ inline float rb(float f){ return b2f(f2b(f)); }
constexpr int S=2048, D=1024, F=3584, H=32, DH=32, W=256;

__global__ void ln_k(const u16* x, u16* xn){
  int row = blockIdx.x*4 + (threadIdx.x>>6), lane = threadIdx.x&63;
  float v[16]; float s=0;
  for(int i=0;i<16;++i){ v[i]=b2f(x[row*D + i*64+lane]); s+=v[i]; }
  for(int o=32;o;o>>=1) s+=__shfl_xor(s,o);
  float m=s/D; float q=0;
  for(int i=0;i<16;++i){ float d=v[i]-m; q+=d*d; }
  for(int o=32;o;o>>=1) q+=__shfl_xor(q,o);
  float mb=rb(m), vb=rb(q/D); float sd=rb(sqrtf(rb(vb+1e-5f)));
  for(int i=0;i<16;++i) xn[row*D+i*64+lane]=f2b(rb(v[i]-mb)/sd);
}
template<int MODE>
__global__ void gemm_k(const u16* A,int lda,const u16* B,int N,int K,u16* C,int ldc,const u16* bias){
  __shared__ float As[32][33], Bs[32][33];
  int tx=threadIdx.x&31, ty=threadIdx.x>>5;
  int m0=blockIdx.y*32, n0=blockIdx.x*32;
  float acc[4]={0,0,0,0};
  for(int k0=0;k0<K;k0+=32){
    for(int i=0;i<4;++i){ As[ty+8*i][tx]=b2f(A[(size_t)(m0+ty+8*i)*lda+k0+tx]); Bs[ty+8*i][tx]=b2f(B[(size_t)(k0+ty+8*i)*N+n0+tx]); }
    __syncthreads();
    for(int k=0;k<32;++k){ float b=Bs[k][tx]; for(int i=0;i<4;++i) acc[i]+=As[ty+8*i][k]*b; }
    __syncthreads();
  }
  for(int i=0;i<4;++i){ float v=rb(acc[i]);
    if(MODE==1){ v=rb(v+b2f(bias[n0+tx])); float u=0.7978845608f*(v+0.044715f*v*v*v); v=0.5f*v*(1.f+tanhf(u)); }
    if(MODE==2){ v=rb(v+b2f(bias[n0+tx])); }
    C[(size_t)(m0+ty+8*i)*ldc+n0+tx]=f2b(v); }
}
__global__ void attn_k(const u16* qkv,u16* o){
  int gw=blockIdx.x*4+(threadIdx.x>>6), lane=threadIdx.x&63; int q=gw/H, h=gw%H;
  float qv[32]; for(int d=0;d<32;++d) qv[d]=b2f(qkv[(size_t)q*3072+h*32+d]);
  float sc[4]; float mx=-1e30f;
  for(int i=0;i<4;++i){ int k=q-(i*64+lane); float s=-1e30f; if(k>=0){ s=0; for(int d=0;d<32;++d) s+=qv[d]*b2f(qkv[(size_t)k*3072+1024+h*32+d]); s=rb(rb(s)*0.17677669529f);} sc[i]=s; mx=fmaxf(mx,s);}
  for(int of=32;of;of>>=1) mx=fmaxf(mx,__shfl_xor(mx,of));
  float l=0; for(int i=0;i<4;++i){ sc[i]=(sc[i]<-1e29f)?0.f:__expf(sc[i]-mx); l+=sc[i]; }
  for(int of=32;of;of>>=1) l+=__shfl_xor(l,of);
  for(int i=0;i<4;++i) sc[i]=rb(sc[i]/l);
  for(int d=0;d<32;++d){ float a=0; for(int i=0;i<4;++i){ int k=q-(i*64+lane); if(k>=0) a+=sc[i]*b2f(qkv[(size_t)k*3072+2048+h*32+d]); }
    for(int of=32;of;of>>=1) a+=__shfl_xor(a,of);
    if(lane==0) o[(size_t)q*D+h*32+d]=f2b(a); }
}
__global__ void fin_k(const u16* x,const u16* a,const u16* b,u16* out){
  int i=blockIdx.x*256+threadIdx.x; out[i]=f2b(rb(b2f(x[i])+b2f(a[i]))+b2f(b[i]));
}
extern "C" void kernel_launch(void* const* d_in, const int* in_sizes, int n_in, void* d_out, int out_size, void* d_ws, size_t ws_size, hipStream_t stream) {
  const u16* x=(const u16*)d_in[0]; const u16* wqkv=(const u16*)d_in[1]; const u16* wo=(const u16*)d_in[2];
  const u16* w1=(const u16*)d_in[4]; const u16* b1=(const u16*)d_in[5]; const u16* w2=(const u16*)d_in[6]; const u16* b2=(const u16*)d_in[7];
  u16* ws=(u16*)d_ws; u16* xn=ws; u16* qkv=xn+S*D; u16* hh=qkv+S*3072; u16* o=hh+S*F; u16* a=o+S*D; u16* b=a+S*D;
  ln_k<<<S/4,256,0,stream>>>(x,xn);
  gemm_k<0><<<dim3(3072/32,S/32),256,0,stream>>>(xn,D,wqkv,3072,D,qkv,3072,nullptr);
  gemm_k<1><<<dim3(F/32,S/32),256,0,stream>>>(xn,D,w1,F,D,hh,F,b1);
  attn_k<<<S*H/4,256,0,stream>>>(qkv,o);
  gemm_k<0><<<dim3(D/32,S/32),256,0,stream>>>(o,D,wo,D,D,a,D,nullptr);
  gemm_k<2><<<dim3(D/32,S/32),256,0,stream>>>(hh,F,w2,D,F,b,D,b2);
  fin_k<<<S*D/256,256,0,stream>>>(x,a,b,(u16*)d_out);
}
```

```cpp
#include <hip/hip_runtime.h>
#include <hip/hip_bf16.h>

typedef unsigned short u16;
typedef short bf16x8 __attribute__((ext_vector_type(8)));
typedef short s16x4 __attribute__((ext_vector_type(4)));
typedef float f32x4 __attribute__((ext_vector_type(4)));
typedef float f32x16 __attribute__((ext_vector_type(16)));
typedef unsigned u32x4 __attribute__((ext_vector_type(4)));
typedef unsigned u32x2 __attribute__((ext_vector_type(2)));
typedef __bf16 bf2_t __attribute__((ext_vector_type(2)));
typedef float f32x2 __attribute__((ext_vector_type(2)));
#define LAS __attribute__((address_space(3)))
#define GAS __attribute__((address_space(1)))
#define DI __device__ __forceinline__
typedef __amdgpu_buffer_rsrc_t rsrc_t;

constexpr int S_ = 2048, D_ = 1024, F_ = 3584, KT_ = F_ + D_;
constexpr size_t OFF_XN = 0;
constexpr size_t OFF_HO = OFF_XN + (size_t)S_ * D_ * 2;
constexpr size_t OFF_KV = OFF_HO + (size_t)S_ * KT_ * 2;
constexpr size_t OFF_PX = OFF_KV + (size_t)128 * 65536;
constexpr size_t OFF_FL = OFF_PX + (size_t)256 * 32768;
constexpr int NFLAG = 128 + 256;
constexpr size_t OFF_ST = OFF_FL + (size_t)NFLAG * 128;
constexpr int LDS_BYTES = 163840;

#define SBAR() __builtin_amdgcn_sched_barrier(0)
#define BAR() asm volatile("s_barrier" ::: "memory")
#define WAITV(n) asm volatile("s_waitcnt vmcnt(" #n ")" ::: "memory")
#define WAITL0() do { __builtin_amdgcn_s_waitcnt(0xC07F); asm volatile("" ::: "memory"); } while (0)
#define WAITVL_BAR(n) do { __builtin_amdgcn_s_waitcnt(0xC07F); asm volatile("s_waitcnt vmcnt(" #n ")\n\ts_barrier" ::: "memory"); } while (0)
#define WAITV_BAR(n) asm volatile("s_waitcnt vmcnt(" #n ")\n\ts_barrier" ::: "memory")
#define PINS(x) asm volatile("" : "+s"(x))
#define MFMA16(a, b, c) __builtin_amdgcn_mfma_f32_16x16x32_bf16(a, b, c, 0, 0, 0)
#define MFMA32(a, b, c) __builtin_amdgcn_mfma_f32_32x32x16_bf16(a, b, c, 0, 0, 0)

#define STAMP(st, i)
#define STAMPC(st, i)

DI float b2f(u16 v) { return __uint_as_float(((unsigned)v) << 16); }
DI float blo(unsigned v) { return __uint_as_float(v << 16); }
DI float bhi(unsigned v) { return __uint_as_float(v & 0xffff0000u); }
DI unsigned pk2(float lo, float hi) { f32x2 v = {lo, hi}; return __builtin_bit_cast(unsigned, __builtin_convertvector(v, bf2_t)); }
DI float rbf(float f) { return blo(pk2(f, 0.f)); }
DI rsrc_t mkrsrc(const void* p, unsigned bytes) { return __builtin_amdgcn_make_buffer_rsrc((void*)p, (short)0, (int)bytes, 0x00020000); }
DI void dma16(unsigned ldsaddr, unsigned voff, rsrc_t srd, unsigned soff) {
    asm volatile("s_mov_b32 m0, %0\n\ts_nop 0\n\tbuffer_load_dwordx4 %1, %2, %3 offen lds" :: "s"(ldsaddr), "v"(voff), "s"(srd), "s"(soff) : "memory");
}
DI bf16x8 lds128(unsigned a) { return *(const LAS bf16x8*)(size_t)a; }
DI s16x4 ldstr(unsigned a) { return __builtin_amdgcn_ds_read_tr16_b64_v4i16((LAS s16x4*)(size_t)a); }
DI bf16x8 cat8(s16x4 lo, s16x4 hi) { return __builtin_shufflevector(lo, hi, 0, 1, 2, 3, 4, 5, 6, 7); }
DI u32x4 pair16(f32x4 a, f32x4 b) {
    unsigned a0 = pk2(a[0], a[1]), a1 = pk2(a[2], a[3]), b0 = pk2(b[0], b[1]), b1 = pk2(b[2], b[3]);
    auto r0 = __builtin_amdgcn_permlane16_swap(a0, b0, false, false);
    auto r1 = __builtin_amdgcn_permlane16_swap(a1, b1, false, false);
    return (u32x4){r0[0], r1[0], r0[1], r1[1]};
}

template <int V> struct IC { static constexpr int value = V; };
template <int N, int I = 0, class Fn> DI void unroll_for(Fn&& f) { if constexpr (I < N) { f(IC<I>{}); unroll_for<N, I + 1>(f); } }

__global__ __launch_bounds__(256) void k0_ln(const u16* __restrict__ x, u16* __restrict__ xn, unsigned* __restrict__ flags) {
    const int lane = threadIdx.x & 63, row = blockIdx.x * 4 + (threadIdx.x >> 6);
    if (threadIdx.x == 0 && blockIdx.x < NFLAG) flags[blockIdx.x * 32] = 0u;
    const u32x4* p = (const u32x4*)(x + (size_t)row * D_);
    u32x4 v0 = p[lane], v1 = p[64 + lane];
    float f[16];
#pragma unroll
    for (int i = 0; i < 4; ++i) { f[2 * i] = blo(v0[i]); f[2 * i + 1] = bhi(v0[i]); f[8 + 2 * i] = blo(v1[i]); f[9 + 2 * i] = bhi(v1[i]); }
    float s = 0.f;
#pragma unroll
    for (int i = 0; i < 16; ++i) s += f[i];
#pragma unroll
    for (int o = 32; o; o >>= 1) s += __shfl_xor(s, o);
    const float m = s * (1.0f / D_);
    float q = 0.f;
#pragma unroll
    for (int i = 0; i < 16; ++i) { const float d = f[i] - m; q += d * d; }
#pragma unroll
    for (int o = 32; o; o >>= 1) q += __shfl_xor(q, o);
    const float mb = rbf(m), sd = rbf(sqrtf(rbf(rbf(q * (1.0f / D_)) + 1e-5f)));
    const float r = 1.0f / sd;
    u32x4 o0, o1;
#pragma unroll
    for (int i = 0; i < 4; ++i) {
        o0[i] = pk2(rbf(f[2 * i] - mb) * r, rbf(f[2 * i + 1] - mb) * r);
        o1[i] = pk2(rbf(f[8 + 2 * i] - mb) * r, rbf(f[9 + 2 * i] - mb) * r);
    }
    u32x4* po = (u32x4*)(xn + (size_t)row * D_);
    po[lane] = o0; po[64 + lane] = o1;
}

constexpr int K1_ASTG = 32768, K1_BOFF = 3 * K1_ASTG;
template <int NF>
DI void k1_mainloop(f32x4 (&acc)[4][NF], unsigned lds0, rsrc_t srdA, unsigned soffA0, rsrc_t srdB, unsigned soffB0, int w, int lane, unsigned long long* st) {
    constexpr int PITCH = 64 * NF;
    constexpr int BSTG = 64 * PITCH;
    constexpr int LDB2 = (NF == 7) ? F_ * 2 : 3 * D_ * 2;
    constexpr int DUMMY = K1_BOFF + 2 * BSTG;
    const int wr = w >> 1, wc = w & 1, fr = lane & 15, fq = lane >> 4;
    const unsigned voffA = (unsigned)((8 * w + (lane >> 3)) * 2048 + (((lane & 7) ^ (4 * (w & 1) + (lane >> 4))) << 4));
    unsigned voffB;
    {
        const int q = 64 * w + lane, row = q / (4 * NF), c = q % (4 * NF);
        if (NF == 7) voffB = (unsigned)(row * LDB2 + ((c ^ (((row >> 3) & 1) << 1)) << 4));
        else { const int s = ((row >> 1) & 1) | (((row >> 3) & 1) << 1); voffB = (unsigned)(row * LDB2 + (c >> 3) * 2048 + (((c & 7) ^ (s << 1)) << 4)); }
        if (w >= NF) voffB = 0x40000000u;
    }
    const unsigned ldsA = lds0 + 1024u * w;
    const unsigned ldsB = (w < NF) ? lds0 + K1_BOFF + 1024u * w : lds0 + DUMMY;
    const unsigned bmul = (w < NF) ? 16u * PITCH : 0u;
#define K1_SETA(v) do { const int u_ = ((v) + 1) >> 1, pr_ = ((v) + 1) & 1, su_ = u_ - 3 * ((u_ * 43) >> 7); \
        const unsigned la_ = ldsA + su_ * K1_ASTG + pr_ * 16384, sa_ = (u_ < 16) ? soffA0 + pr_ * (128 * 2048) + u_ * 128 : 0x40000000u; \
        dma16(la_, voffA, srdA, sa_); dma16(la_ + 8192, voffA, srdA, sa_ + 64 * 2048); } while (0)
#define K1_SETB(v) do { const unsigned lb_ = ldsB + bmul * (2 * ((v) & 3)), sb_ = soffB0 + (v) * (32 * LDB2); \
        dma16(lb_, voffB, srdB, sb_); dma16(lb_ + bmul, voffB, srdB, sb_ + 16 * LDB2); } while (0)
    const unsigned ra0 = lds0 + (64 * wr + fr) * 128 + ((fq ^ (fr >> 1)) << 4), ra1 = ra0 ^ 64u;
    unsigned rb[4];
    {
        const int g = lane >> 4, q = (lane & 15) >> 2, p = lane & 3;
        const unsigned rowp = lds0 + K1_BOFF + (8 * g + q) * PITCH + 8 * p;
        if (NF == 7) {
            const unsigned e = rowp + (g & 1) * 32, o = rowp - (g & 1) * 32;
            rb[0] = wc ? o + 7 * 32 : e; rb[1] = wc ? e + 7 * 32 : o; rb[2] = rb[0]; rb[3] = rb[1];
        } else {
            const int s = (q >> 1) | ((g & 1) << 1);
#pragma unroll
            for (int i = 0; i < 4; ++i) { const int f = 6 * wc + i; rb[i] = rowp + (f >> 2) * 128 + (((f & 3) ^ s) << 5); }
        }
    }
    bf16x8 xf[2][4]; s16x4 wl[2][NF], wh[2][NF];
    unsigned ca, cb[4];
#define K1_ADDR(st, slot, ks) do { ca = ((ks) ? ra1 : ra0) + (st) * K1_ASTG; \
        _Pragma("unroll") for (int i = 0; i < (NF == 7 ? 2 : 4); ++i) cb[i] = rb[i] + (slot) * (32 * PITCH); \
        asm volatile("" : "+v"(ca), "+v"(cb[0]), "+v"(cb[1])); if (NF != 7) asm volatile("" : "+v"(cb[2]), "+v"(cb[3])); } while (0)
#define K1_RD1(S, r) do { constexpr int n_ = ((r) < 12) ? (r) / 3 : 4 + ((r) - 12) / 2, k_ = ((r) < 12) ? (r) % 3 : ((r) - 12) % 2; \
        const unsigned b_ = (NF == 7) ? cb[n_ & 1] + 32 * n_ : cb[n_ & 3] + (n_ >> 2) * 128; \
        if (k_ == 0) wl[S][n_] = ldstr(b_); else if (k_ == 1) wh[S][n_] = ldstr(b_ + 4 * PITCH); else xf[S][n_ & 3] = lds128(ca + (n_ & 3) * 2048); } while (0)
#define K1_D(ADDR) do { \
        dma16(lb, voffB, srdB, sb); \
        const unsigned lb1_ = lb + bmul, sb1_ = sb + 16 * LDB2, la0_ = la, sa0_ = (v < 31) ? sa : 0x40000000u; \
        dma16(lb1_, voffB, srdB, sb1_); \
        lb += 2 * bmul; lb = (lb == lbend) ? ldsB : lb; sb += 32 * LDB2; PINS(lb); PINS(sb); ADDR; \
        dma16(la0_, voffA, srdA, sa0_); \
        la += 16384; la = (la == laend) ? ldsA : la; sa += da; da = 128 - da; ++v; PINS(la); PINS(sa); PINS(da); PINS(v); \
        dma16(la0_ + 8192, voffA, srdA, sa0_ + 64 * 2048); } while (0)
#define K1_M(S) do { SBAR(); \
        unroll_for<4 * NF>([&](auto J) { constexpr int j = J.value, n = j >> 2, m = (n & 1) ? 3 - (j & 3) : (j & 3); \
            acc[m][n] = MFMA16(cat8(wl[S][n], wh[S][n]), xf[S][m], acc[m][n]); \
            if constexpr (j < 4 + 2 * NF) K1_RD1((S) ^ 1, j); SBAR(); }); \
        WAITL0(); BAR(); SBAR(); } while (0)
    K1_SETA(-1); K1_SETB(0); K1_SETA(0); K1_SETB(1); K1_SETA(1); K1_SETB(2); K1_SETA(2);
    K1_ADDR(0, 0, 0);
    WAITVL_BAR(8);
    unroll_for<4 + 2 * NF>([&](auto J) { K1_RD1(0, J.value); });
    int v = 3;
    const unsigned lbend = ldsB + 8 * bmul, laend = ldsA + 3 * K1_ASTG;
    unsigned lb = ldsB + 6 * bmul, sb = soffB0 + 3 * 32 * LDB2, la = ldsA + 2 * K1_ASTG, sa = soffA0 + 2 * 128, da = 128 * 2048;
    if (w >= 4) { K1_D(); WAITVL_BAR(8); }
    STAMPC(st, 10);
    unsigned rs = 0, rq = 1;
    for (int t = 0; t < 16; ++t) {
        K1_D(K1_ADDR(rs, rq, 1); rs = (rs == 2) ? 0 : rs + 1; rq = (rq + 1) & 3; PINS(rs); PINS(rq)); WAITVL_BAR(8);
        K1_M(0);
        K1_D(K1_ADDR(rs, rq, 0); rq = rq + 1; PINS(rq)); WAITV_BAR(8);
        K1_M(1);
    }
    if (w < 4) BAR();
    WAITVL_BAR(0);
#undef K1_SETA
#undef K1_SETB
#undef K1_ADDR
#undef K1_RD1
#undef K1_D
#undef K1_M
}

DI float gelu_tanh(float v) {
    const float c0 = -2.0f * 0.7978845608028654f * 1.4426950408889634f, c1 = c0 * 0.044715f;
    const float e = __builtin_amdgcn_exp2f(v * __builtin_fmaf(v * v, c1, c0));
    return v * __builtin_amdgcn_rcpf(1.0f + e);
}

DI int crow(int r, int hi) { return (r & 3) + 8 * (r >> 2) + 4 * hi; }
constexpr int AT_K = 0, AT_V = 65536, AT_Q = 131072;

template <int MODE> DI void at_mask(f32x16& s, int ql, int hi) {
    if (MODE == 0) return;
#pragma unroll
    for (int r = 0; r < 16; ++r) { const int k = crow(r, hi); const bool keep = (MODE == 1) ? (k <= ql) : (k > ql); s[r] = keep ? s[r] : -INFINITY; }
}
template <int MODE> DI float at_max_tile(unsigned ka0, unsigned ka1, bf16x8 q0, bf16x8 q1, float mx, int ql, int hi) {
    f32x16 s = {};
    s = MFMA32(lds128(ka0), q0, s); s = MFMA32(lds128(ka1), q1, s);
    at_mask<MODE>(s, ql, hi);
#pragma unroll
    for (int r = 0; r < 16; r += 2) mx = __builtin_fmaxf(__builtin_fmaxf(mx, s[r]), s[r + 1]);
    return mx;
}
template <int MODE> DI void at_pv_tile(unsigned ka0, unsigned ka1, unsigned va0, unsigned va1, bf16x8 q0, bf16x8 q1, const f32x16& negm, float& l, f32x16& o, int ql, int hi) {
    f32x16 s = MFMA32(lds128(ka0), q0, negm); s = MFMA32(lds128(ka1), q1, s);
    const bf16x8 v0 = cat8(ldstr(va0), ldstr(va1)), v1 = cat8(ldstr(va0 + 1024), ldstr(va1 + 1024));
    at_mask<MODE>(s, ql, hi);
    float la = 0.f, lb = 0.f;
#pragma unroll
    for (int r = 0; r < 16; r += 2) { s[r] = __builtin_amdgcn_exp2f(s[r]); s[r + 1] = __builtin_amdgcn_exp2f(s[r + 1]); la += s[r]; lb += s[r + 1]; }
    l += la + lb;
    const u32x4 p0 = {pk2(s[0], s[1]), pk2(s[2], s[3]), pk2(s[4], s[5]), pk2(s[6], s[7])};
    const u32x4 p1 = {pk2(s[8], s[9]), pk2(s[10], s[11]), pk2(s[12], s[13]), pk2(s[14], s[15])};
    o = MFMA32(v0, __builtin_bit_cast(bf16x8, p0), o); o = MFMA32(v1, __builtin_bit_cast(bf16x8, p1), o);
}
DI float xhalf_max(float m) { auto r = __builtin_amdgcn_permlane32_swap(__float_as_uint(m), __float_as_uint(m), false, false); return __builtin_fmaxf(__uint_as_float(r[0]), __uint_as_float(r[1])); }
DI float xhalf_sum(float m) { auto r = __builtin_amdgcn_permlane32_swap(__float_as_uint(m), __float_as_uint(m), false, false); return __uint_as_float(r[0]) + __uint_as_float(r[1]); }
DI f32x16 splat16(float v) { f32x16 r; _Pragma("unroll") for (int i = 0; i < 16; ++i) r[i] = v; return r; }

DI void at_stage1(int t, unsigned ka0, unsigned ka1, unsigned va0, unsigned va1, bf16x8 q0, bf16x8 q1, float& m, float& l, f32x16& o, int ql, int hi) {
    float mx = -INFINITY;
    for (int kt = 8; kt < 8 + t; ++kt) mx = at_max_tile<0>(ka0 + kt * 2048, ka1 + kt * 2048, q0, q1, mx, ql, hi);
    mx = at_max_tile<1>(ka0 + (8 + t) * 2048, ka1 + (8 + t) * 2048, q0, q1, mx, ql, hi);
    m = xhalf_max(mx);
    const f32x16 negm = splat16(-m);
    l = 0.f; o = splat16(0.f);
    for (int kt = 8; kt < 8 + t; ++kt) at_pv_tile<0>(ka0 + kt * 2048, ka1 + kt * 2048, va0 + kt * 2048, va1 + kt * 2048, q0, q1, negm, l, o, ql, hi);
    at_pv_tile<1>(ka0 + (8 + t) * 2048, ka1 + (8 + t) * 2048, va0 + (8 + t) * 2048, va1 + (8 + t) * 2048, q0, q1, negm, l, o, ql, hi);
}
DI void at_stage2(int t, unsigned ka0, unsigned ka1, unsigned va0, unsigned va1, bf16x8 q0, bf16x8 q1, float& m, float& l, f32x16& o, int ql, int hi) {
    float mx = at_max_tile<2>(ka0 + t * 2048, ka1 + t * 2048, q0, q1, m, ql, hi);
    for (int kt = t + 1; kt < 8; ++kt) mx = at_max_tile<0>(ka0 + kt * 2048, ka1 + kt * 2048, q0, q1, mx, ql, hi);
    const float mn = xhalf_max(mx), al = __builtin_amdgcn_exp2f(m - mn);
    m = mn; l *= al;
#pragma unroll
    for (int r = 0; r < 16; ++r) o[r] *= al;
    const f32x16 negm = splat16(-mn);
    at_pv_tile<2>(ka0 + t * 2048, ka1 + t * 2048, va0 + t * 2048, va1 + t * 2048, q0, q1, negm, l, o, ql, hi);
    for (int kt = t + 1; kt < 8; ++kt) at_pv_tile<0>(ka0 + kt * 2048, ka1 + kt * 2048, va0 + kt * 2048, va1 + kt * 2048, q0, q1, negm, l, o, ql, hi);
}
DI void at_store(u16* orow, float l, const f32x16& o, int hi) {
    const float rl = __builtin_amdgcn_rcpf(xhalf_sum(l));
#pragma unroll
    for (int k = 0; k < 4; k += 2) {
        unsigned a0 = pk2(o[4 * k] * rl, o[4 * k + 1] * rl), a1 = pk2(o[4 * k + 2] * rl, o[4 * k + 3] * rl);
        unsigned b0 = pk2(o[4 * k + 4] * rl, o[4 * k + 5] * rl), b1 = pk2(o[4 * k + 6] * rl, o[4 * k + 7] * rl);
        auto r0 = __builtin_amdgcn_permlane32_swap(a0, b0, false, false);
        auto r1 = __builtin_amdgcn_permlane32_swap(a1, b1, false, false);
        *(u32x4*)(orow + 8 * k + 8 * hi) = (u32x4){r0[0], r1[0], r0[1], r1[1]};
    }
}

DI void wait_count(unsigned* flag, unsigned want) {
    GAS unsigned* f = (GAS unsigned*)flag;
    for (unsigned spins = 0; spins < (1u << 22); ++spins) {
        if ((unsigned)__builtin_amdgcn_readfirstlane(__hip_atomic_load(f, __ATOMIC_RELAXED, __HIP_MEMORY_SCOPE_AGENT)) >= want) break;
        __builtin_amdgcn_s_sleep(1);
    }
    __builtin_amdgcn_fence(__ATOMIC_ACQUIRE, "wavefront");
}

__global__ __launch_bounds__(512, 2) void k1_qkv_attn_ffn1(const u16* __restrict__ xn, const u16* __restrict__ wqkv, const u16* __restrict__ w1, const u16* __restrict__ b1,
                                                          u16* __restrict__ ho, u16* __restrict__ kvx, unsigned* __restrict__ flags, unsigned long long* __restrict__ st) {
    extern __shared__ __attribute__((aligned(16))) char smem[];
    const unsigned lds0 = (unsigned)(size_t)(LAS char*)smem;
    const int lane = threadIdx.x & 63, w = __builtin_amdgcn_readfirstlane(threadIdx.x >> 6);
    const int xcd = blockIdx.x & 7, idx = blockIdx.x >> 3;
    const int pm = 4 * (xcd & 1) + (idx & 3), j = 4 * (xcd >> 1) + ((idx >> 2) & 3);
    const int wr = w >> 1, wc = w & 1, fr = lane & 15, fq = lane >> 4;
    const rsrc_t srdA = mkrsrc(xn, S_ * D_ * 2);
    STAMP(st, 0); STAMPC(st, 8);
    if (idx >= 16) {
        const int col0 = 224 * j + 112 * wc;
        u32x2 bv[7];
#pragma unroll
        for (int n = 0; n < 7; ++n) bv[n] = *(const u32x2*)(b1 + col0 + 16 * n + 4 * fq);
        f32x4 acc[4][7];
#pragma unroll
        for (int m = 0; m < 4; ++m)
#pragma unroll
            for (int n = 0; n < 7; ++n) acc[m][n] = (f32x4){0.f, 0.f, 0.f, 0.f};
        k1_mainloop<7>(acc, lds0, srdA, (unsigned)(pm * 256 * 2048), mkrsrc(w1, D_ * F_ * 2), (unsigned)(j * 448), w, lane, st);
        STAMP(st, 1); STAMPC(st, 9);
#pragma unroll
        for (int m = 0; m < 4; ++m)
#pragma unroll
            for (int n = 0; n < 7; ++n) {
                acc[m][n][0] = gelu_tanh(acc[m][n][0] + blo(bv[n][0])); acc[m][n][1] = gelu_tanh(acc[m][n][1] + bhi(bv[n][0]));
                acc[m][n][2] = gelu_tanh(acc[m][n][2] + blo(bv[n][1])); acc[m][n][3] = gelu_tanh(acc[m][n][3] + bhi(bv[n][1]));
            }
        u16* base = ho + (size_t)(pm * 256 + 64 * wr + fr) * KT_ + col0;
#pragma unroll
        for (int m = 0; m < 4; ++m)
#pragma unroll
            for (int y = 0; y < 3; ++y) *(u32x4*)(base + (size_t)(16 * m) * KT_ + 32 * y + 16 * (fq & 1) + 8 * (fq >> 1)) = pair16(acc[m][2 * y], acc[m][2 * y + 1]);
#pragma unroll
        for (int m = 0; m < 4; m += 2) *(u32x4*)(base + (size_t)(16 * (m + (fq & 1))) * KT_ + 96 + 8 * (fq >> 1)) = pair16(acc[m][6], acc[m + 1][6]);
        STAMP(st, 2);
        return;
    }
    f32x4 acc[4][6];
#pragma unroll
    for (int m = 0; m < 4; ++m)
#pragma unroll
        for (int n = 0; n < 6; ++n) acc[m][n] = (f32x4){0.f, 0.f, 0.f, 0.f};
    k1_mainloop<6>(acc, lds0, srdA, (unsigned)(pm * 256 * 2048), mkrsrc(wqkv, D_ * 3 * D_ * 2), (unsigned)(j * 128), w, lane, st);
    STAMP(st, 1); STAMPC(st, 9);
    {
        const rsrc_t srdX = mkrsrc(kvx + (size_t)(pm * 16 + j) * 32768, 65536);
        const float qs = wc ? 1.0f : 0.17677669529663687f * 1.4426950408889634f;
        const unsigned rowoff = (unsigned)((64 * wr + fr) * 64 + (((2 * (fq & 1) + (fq >> 1)) ^ ((fr >> 2) & 3)) << 4));
#pragma unroll
        for (int y = 2; y >= 0; --y) {
            const int blk = 3 * wc + y;
            const unsigned lb = lds0 + (blk < 2 ? AT_Q + blk * 16384 : (blk - 2) * 32768 + 16384) + rowoff;
            const float sc = (y == 2) ? 1.0f : qs;
#pragma unroll
            for (int m = 0; m < 4; ++m) {
                const u32x4 v = pair16(acc[m][2 * y] * sc, acc[m][2 * y + 1] * sc);
                if (blk >= 2 && pm < 7) __builtin_amdgcn_raw_buffer_store_b128(v, srdX, rowoff + m * 1024, (blk - 2) * 16384, 16);
                *(LAS u32x4*)(size_t)(lb + m * 1024) = v;
            }
        }
    }
    WAITL0(); BAR();
    const int hd = w >> 2, ta = w & 3, tb = 7 - ta, ql = lane & 31, hi = lane >> 5;
    const unsigned frag = (unsigned)(ql * 64 + ((hi ^ ((ql >> 2) & 3)) << 4));
    const unsigned ka0 = lds0 + AT_K + hd * 32768 + frag, ka1 = ka0 ^ 32u;
    const unsigned qa = lds0 + AT_Q + hd * 16384 + frag;
    const bf16x8 qa0 = lds128(qa + ta * 2048), qa1 = lds128((qa ^ 32u) + ta * 2048), qb0 = lds128(qa + tb * 2048), qb1 = lds128((qa ^ 32u) + tb * 2048);
    unsigned va0, va1;
    {
        const int blk = (lane >> 4) & 1, q = (lane & 15) >> 2, p = lane & 3, c = 2 * blk + (p >> 1);
        const unsigned vb = lds0 + AT_V + hd * 32768 + (4 * hi + q) * 64 + 8 * (p & 1);
        va0 = vb + ((c ^ hi) << 4); va1 = vb + 512 + ((c ^ (2 + hi)) << 4);
    }
    float ma, la, mb, lb; f32x16 oa, ob;
    at_stage1(ta, ka0, ka1, va0, va1, qa0, qa1, ma, la, oa, ql, hi);
    if (pm < 7) {
        WAITV(0);
        if (lane == 0) __hip_atomic_fetch_add((GAS unsigned*)(flags + (pm * 16 + j) * 32), 1u, __ATOMIC_RELAXED, __HIP_MEMORY_SCOPE_AGENT);
    }
    at_stage1(tb, ka0, ka1, va0, va1, qb0, qb1, mb, lb, ob, ql, hi);
    STAMP(st, 2);
    if (pm > 0) {
        if (w == 0) wait_count(flags + ((pm - 1) * 16 + j) * 32, 8u);
        BAR();
        STAMP(st, 3);
        const rsrc_t srdX = mkrsrc(kvx + (size_t)((pm - 1) * 16 + j) * 32768, 65536);
        u32x4 t[8];
#pragma unroll
        for (int i = 0; i < 8; ++i) t[i] = __builtin_amdgcn_raw_buffer_load_b128(srdX, w * 8192 + i * 1024 + lane * 16, 0, 16);
        const unsigned lb2 = lds0 + (w >> 1) * 32768 + (w & 1) * 8192 + lane * 16;
#pragma unroll
        for (int i = 0; i < 8; ++i) *(LAS u32x4*)(size_t)(lb2 + i * 1024) = t[i];
        WAITL0(); BAR();
        STAMP(st, 4);
        at_stage2(ta, ka0, ka1, va0, va1, qa0, qa1, ma, la, oa, ql, hi);
        at_stage2(tb, ka0, ka1, va0, va1, qb0, qb1, mb, lb, ob, ql, hi);
    }
    u16* ob_ = ho + (size_t)(pm * 256 + ql) * KT_ + F_ + 64 * j + 32 * hd;
    at_store(ob_ + (size_t)(32 * ta) * KT_, la, oa, hi);
    at_store(ob_ + (size_t)(32 * tb) * KT_, lb, ob, hi);
    STAMP(st, 5);
}

__global__ __launch_bounds__(512, 2) void k2_out(const u16* __restrict__ ho, const u16* __restrict__ w2, const u16* __restrict__ wo, const u16* __restrict__ x, const u16* __restrict__ b2,
                                                u16* __restrict__ out, float* __restrict__ px, unsigned* __restrict__ flags, unsigned long long* __restrict__ st) {
    extern __shared__ __attribute__((aligned(16))) char smem[];
    const unsigned lds0 = (unsigned)(size_t)(LAS char*)smem;
    const int lane = threadIdx.x & 63, w = __builtin_amdgcn_readfirstlane(threadIdx.x >> 6);
    const int xcd = blockIdx.x & 7, idx = blockIdx.x >> 3;
    const int h = xcd & 1, rt = 8 * ((xcd >> 1) & 1) + (idx & 7), ct = 4 * (xcd >> 2) + (idx >> 3);
    const int g = w >> 2, w4 = w & 3, wr = w4 >> 1, wc = w4 & 1, fr = lane & 15, fq = lane >> 4;
    STAMP(st, 0); STAMPC(st, 8);
    const rsrc_t srdA = mkrsrc(ho, S_ * KT_ * 2), srdW2 = mkrsrc(w2, F_ * D_ * 2), srdWo = mkrsrc(wo, F_ * D_ * 2);
    const unsigned voffA = (unsigned)((8 * w4 + (lane >> 3)) * (KT_ * 2) + (((lane & 7) ^ (4 * (w4 & 1) + (lane >> 4))) << 4));
    const unsigned voffB = (unsigned)((4 * w4 + (lane >> 4)) * 2048 + (((lane & 15) ^ (((lane >> 4) | ((w4 >> 1) << 2)) << 1)) << 4));
    const unsigned soffA0 = (unsigned)(rt * 128 * (KT_ * 2) + h * 36 * 128), soffB0 = (unsigned)(ct * 256);
    const unsigned ldsW = lds0 + 1024u * w4;
#define K2_ST(tau, buf) do { const int kt_ = 36 * h + (tau); const bool o_ = kt_ >= 56; const rsrc_t sb_ = o_ ? srdWo : srdW2; \
        const unsigned sb0_ = soffB0 + (unsigned)((o_ ? kt_ - 56 : kt_) * (64 * 2048)), l_ = ldsW + (buf) * 32768; \
        _Pragma("unroll") for (int i = 0; i < 4; ++i) { \
            dma16(l_ + 16384 + i * 4096, voffB, sb_, sb0_ + i * (16 * 2048)); \
            dma16(l_ + i * 4096, voffA, srdA, soffA0 + i * (32 * KT_ * 2) + (tau) * 128); } } while (0)
    const unsigned ra0 = lds0 + (64 * wr + fr) * 128 + ((fq ^ (fr >> 1)) << 4), ra1 = ra0 ^ 64u;
    unsigned rb[4];
    {
        const int q = (lane & 15) >> 2, p = lane & 3, s = q | ((fq & 1) << 2);
#pragma unroll
        for (int n = 0; n < 4; ++n) rb[n] = lds0 + 16384 + (8 * fq + q) * 256 + (((4 * wc + n) ^ s) << 5) + 8 * p;
    }
    f32x4 acc[4][4];
#pragma unroll
    for (int m = 0; m < 4; ++m)
#pragma unroll
        for (int n = 0; n < 4; ++n) acc[m][n] = (f32x4){0.f, 0.f, 0.f, 0.f};
    bf16x8 xf[2][2][4]; s16x4 wl[2][2][4], wh[2][2][4];
    unsigned ca0, ca1, cb[4];
#define K2_ADDR(buf) do { const unsigned o_ = (buf) * 32768; ca0 = ra0 + o_; ca1 = ra1 + o_; _Pragma("unroll") for (int i = 0; i < 4; ++i) cb[i] = rb[i] + o_; \
        asm volatile("" : "+v"(ca0), "+v"(ca1), "+v"(cb[0]), "+v"(cb[1]), "+v"(cb[2]), "+v"(cb[3])); } while (0)
#define K2_RD1(S, r) do { constexpr int f_ = (r) / 3, k_ = f_ >> 2, n_ = f_ & 3; \
        if ((r) % 3 == 0) wl[S][k_][n_] = ldstr(cb[n_] + k_ * 8192); else if ((r) % 3 == 1) wh[S][k_][n_] = ldstr(cb[n_] + k_ * 8192 + 1024); \
        else xf[S][k_][n_] = lds128((k_ ? ca1 : ca0) + n_ * 2048); } while (0)
#define K2_MM(S) do { SBAR(); \
        unroll_for<32>([&](auto J) { constexpr int j = J.value, ks = j >> 4, n = (j >> 2) & 3, m = (n & 1) ? 3 - (j & 3) : (j & 3); \
            acc[m][n] = MFMA16(cat8(wl[S][ks][n], wh[S][ks][n]), xf[S][ks][m], acc[m][n]); \
            if constexpr (j < 24) K2_RD1((S) ^ 1, j); SBAR(); }); } while (0)
#define K2_STEP(S) do { \
        const unsigned l_ = lw, sa_ = sa, sb_ = sb; const rsrc_t rb_ = srdB; \
        dma16(l_ + 16384, voffB, rb_, sb_); \
        dma16(l_, voffA, srdA, sa_); \
        bc = (bc >= 3) ? bc - 3 : bc + 2; PINS(bc); K2_ADDR(bc); \
        dma16(l_ + 16384 + 4096, voffB, rb_, sb_ + 16 * 2048); \
        kt += 2; lw = ldsW + bc * 32768; sa = (kt < ktend) ? sa + 256 : 0x40000000u; PINS(kt); PINS(lw); PINS(sa); \
        dma16(l_ + 4096, voffA, srdA, sa_ + 32 * KT_ * 2); \
        sb = (kt < ktend) ? soffB0 + (unsigned)((kt >= 56 ? kt - 56 : kt) * (64 * 2048)) : 0x40000000u; srdB = (kt >= 56) ? srdWo : srdW2; PINS(sb); PINS(srdB); \
        dma16(l_ + 16384 + 8192, voffB, rb_, sb_ + 32 * 2048); \
        dma16(l_ + 8192, voffA, srdA, sa_ + 64 * KT_ * 2); \
        dma16(l_ + 16384 + 12288, voffB, rb_, sb_ + 48 * 2048); \
        dma16(l_ + 12288, voffA, srdA, sa_ + 96 * KT_ * 2); \
        BAR(); K2_MM(S); WAITVL_BAR(8); SBAR(); } while (0)
    if (g) { K2_ST(0, 0); K2_ST(2, 2); K2_ST(4, 4); WAITV(16); } else { K2_ST(1, 1); K2_ST(3, 3); WAITV(8); }
    K2_ADDR(g);
    BAR();
    unroll_for<24>([&](auto J) { K2_RD1(0, J.value); });
    WAITL0(); BAR();
    if (g) WAITVL_BAR(8);
    STAMPC(st, 10);
    int bc = g, kt = 36 * h + g + 5;
    const int ktend = 36 * h + 36;
    unsigned lw = ldsW + g * 32768, sa = soffA0 + (g + 5) * 128, sb = soffB0 + (unsigned)(kt * (64 * 2048));
    rsrc_t srdB = srdW2;
    for (int it = 0; it < 8; ++it) { K2_STEP(0); K2_STEP(1); }
    bc = (bc >= 3) ? bc - 3 : bc + 2; K2_ADDR(bc); BAR(); K2_MM(0); WAITVL_BAR(0); SBAR();
    BAR(); K2_MM(1); WAITL0(); BAR(); SBAR();
    if (!g) BAR();
    STAMP(st, 1); STAMPC(st, 9);
    const int row = rt * 128 + 64 * wr + 16 * (2 * g + h) + fr, col = ct * 128 + 64 * wc;
    u32x2 xv[4], bv[4];
#pragma unroll
    for (int n = 0; n < 4; ++n) { xv[n] = *(const u32x2*)(x + (size_t)row * D_ + col + 16 * n + 4 * fq); bv[n] = *(const u32x2*)(b2 + col + 16 * n + 4 * fq); }
    {
        const unsigned wa = lds0 + w * 8192 + lane * 16;
#pragma unroll
        for (int n = 0; n < 4; ++n) {
            *(LAS f32x4*)(size_t)(wa + n * 1024) = g ? acc[0][n] : acc[2][n];
            *(LAS f32x4*)(size_t)(wa + 4096 + n * 1024) = g ? acc[1][n] : acc[3][n];
        }
    }
    WAITL0(); BAR();
    f32x4 keep[4], send[4];
    {
        const unsigned rbase = lds0 + (w ^ 4) * 8192 + lane * 16;
#pragma unroll
        for (int n = 0; n < 4; ++n) {
            const f32x4 lo = g ? acc[2][n] : acc[0][n], hi = g ? acc[3][n] : acc[1][n];
            send[n] = (h ? lo : hi) + *(const LAS f32x4*)(size_t)(rbase + (1 - h) * 4096 + n * 1024);
            keep[n] = (h ? hi : lo) + *(const LAS f32x4*)(size_t)(rbase + h * 4096 + n * 1024);
        }
    }
    const int tile = rt * 8 + ct;
    {
        const rsrc_t srdS = mkrsrc(px + (size_t)(tile * 2 + h) * 8192, 32768);
#pragma unroll
        for (int n = 0; n < 4; ++n) __builtin_amdgcn_raw_buffer_store_b128(__builtin_bit_cast(u32x4, send[n]), srdS, w * 4096 + n * 1024 + lane * 16, 0, 16);
        WAITV(0);
        if (lane == 0) __hip_atomic_fetch_add((GAS unsigned*)(flags + (128 + tile * 2 + h) * 32), 1u, __ATOMIC_RELAXED, __HIP_MEMORY_SCOPE_AGENT);
    }
    STAMP(st, 2);
    if (w == 0) wait_count(flags + (128 + tile * 2 + (1 - h)) * 32, 8u);
    BAR();
    STAMP(st, 3);
    {
        const rsrc_t srdR = mkrsrc(px + (size_t)(tile * 2 + (1 - h)) * 8192, 32768);
#pragma unroll
        for (int n = 0; n < 4; ++n) {
            const f32x4 r = __builtin_bit_cast(f32x4, __builtin_amdgcn_raw_buffer_load_b128(srdR, w * 4096 + n * 1024 + lane * 16, 0, 16));
            keep[n] += r + (f32x4){blo(xv[n][0]) + blo(bv[n][0]), bhi(xv[n][0]) + bhi(bv[n][0]), blo(xv[n][1]) + blo(bv[n][1]), bhi(xv[n][1]) + bhi(bv[n][1])};
        }
    }
    u16* op = out + (size_t)row * D_ + col + 16 * (fq & 1) + 8 * (fq >> 1);
    *(u32x4*)op = pair16(keep[0], keep[1]);
    *(u32x4*)(op + 32) = pair16(keep[2], keep[3]);
    STAMP(st, 4);
}

extern "C" void kernel_launch(void* const* d_in, const int* in_sizes, int n_in, void* d_out, int out_size, void* d_ws, size_t ws_size, hipStream_t stream) {
    const u16* x = (const u16*)d_in[0]; const u16* wqkv = (const u16*)d_in[1]; const u16* wo = (const u16*)d_in[2];
    const u16* w1 = (const u16*)d_in[4]; const u16* b1 = (const u16*)d_in[5]; const u16* w2 = (const u16*)d_in[6]; const u16* b2 = (const u16*)d_in[7];
    char* ws = (char*)d_ws;
    u16* xn = (u16*)(ws + OFF_XN); u16* ho = (u16*)(ws + OFF_HO); u16* kvx = (u16*)(ws + OFF_KV); float* px = (float*)(ws + OFF_PX);
    unsigned* flags = (unsigned*)(ws + OFF_FL); unsigned long long* st = (unsigned long long*)(ws + OFF_ST);
    hipFuncSetAttribute((const void*)k1_qkv_attn_ffn1, hipFuncAttributeMaxDynamicSharedMemorySize, LDS_BYTES);
    hipFuncSetAttribute((const void*)k2_out, hipFuncAttributeMaxDynamicSharedMemorySize, LDS_BYTES);
    k0_ln<<<S_ / 4, 256, 0, stream>>>(x, xn, flags);
    k1_qkv_attn_ffn1<<<256, 512, LDS_BYTES, stream>>>(xn, wqkv, w1, b1, ho, kvx, flags, st);
    k2_out<<<256, 512, LDS_BYTES, stream>>>(ho, w2, wo, x, b2, (u16*)d_out, px, flags, st + 256 * 2 * 16);
}
```

```cpp
#include <hip/hip_runtime.h>
#include <hip/hip_bf16.h>

typedef unsigned short u16;
typedef unsigned long long u64;
typedef short bf16x8 __attribute__((ext_vector_type(8)));
typedef short s16x4 __attribute__((ext_vector_type(4)));
typedef float f32x4 __attribute__((ext_vector_type(4)));
typedef float f32x16 __attribute__((ext_vector_type(16)));
typedef unsigned u32x4 __attribute__((ext_vector_type(4)));
typedef unsigned u32x2 __attribute__((ext_vector_type(2)));
typedef __bf16 bf2_t __attribute__((ext_vector_type(2)));
typedef float f32x2 __attribute__((ext_vector_type(2)));
#define LAS __attribute__((address_space(3)))
#define GAS __attribute__((address_space(1)))
#define DI __device__ __forceinline__
typedef __amdgpu_buffer_rsrc_t rsrc_t;

constexpr int S_ = 2048, D_ = 1024, F_ = 3584, KT_ = F_ + D_;
constexpr size_t OFF_HO = (size_t)S_ * D_ * 2;
constexpr size_t OFF_KV = OFF_HO + (size_t)S_ * KT_ * 2;
constexpr size_t OFF_PX = OFF_KV + (size_t)128 * 65536;
constexpr size_t OFF_FL = OFF_PX + (size_t)256 * 32768;
constexpr int FL_KV = 32, FL_P1 = FL_KV + 128 * 16, FL_RS = FL_P1 + 256;
constexpr size_t OFF_ST = OFF_FL + (size_t)384 * 128;
constexpr int LDS_BYTES = 163840;
constexpr unsigned OOB = 0x40000000u;

#define SBAR() __builtin_amdgcn_sched_barrier(0)
#define BAR() asm volatile("s_barrier" ::: "memory")
#define WAITV(n) asm volatile("s_waitcnt vmcnt(" #n ")" ::: "memory")
#define WAITL0() do { __builtin_amdgcn_s_waitcnt(0xC07F); asm volatile("" ::: "memory"); } while (0)
#define WAITVL_BAR(n) do { __builtin_amdgcn_s_waitcnt(0xC07F); asm volatile("s_waitcnt vmcnt(" #n ")\n\ts_barrier" ::: "memory"); } while (0)
#define WAITV_BAR(n) asm volatile("s_waitcnt vmcnt(" #n ")\n\ts_barrier" ::: "memory")
#define PINS(x) asm volatile("" : "+s"(x))
#define PINV(x) asm volatile("" : "+v"(x))
#define MFMA16(a, b, c) __builtin_amdgcn_mfma_f32_16x16x32_bf16(a, b, c, 0, 0, 0)
#define MFMA32(a, b, c) __builtin_amdgcn_mfma_f32_32x32x16_bf16(a, b, c, 0, 0, 0)
#define RLX_AGENT __ATOMIC_RELAXED, __HIP_MEMORY_SCOPE_AGENT
#define BF8(x) __builtin_bit_cast(bf16x8, x)

#define STAMP(k, i)
#define STAMPC(k, i)

DI float blo(unsigned v) { return __uint_as_float(v << 16); }
DI float bhi(unsigned v) { return __uint_as_float(v & 0xffff0000u); }
DI unsigned pk2(float lo, float hi) { f32x2 v = {lo, hi}; return __builtin_bit_cast(unsigned, __builtin_convertvector(v, bf2_t)); }
DI float rbf(float f) { return blo(pk2(f, 0.f)); }
DI rsrc_t mkrsrc(const void* p, unsigned bytes) { return __builtin_amdgcn_make_buffer_rsrc((void*)p, (short)0, (int)bytes, 0x00020000); }
DI void dma16(unsigned ldsaddr, unsigned voff, rsrc_t srd, unsigned soff) {
    asm volatile("s_mov_b32 m0, %0\n\ts_nop 0\n\tbuffer_load_dwordx4 %1, %2, %3 offen lds" :: "s"(ldsaddr), "v"(voff), "s"(srd), "s"(soff) : "memory");
}
DI void dma16c(unsigned ldsaddr, unsigned voff, rsrc_t srd, unsigned soff) {
    asm volatile("s_mov_b32 m0, %0\n\ts_nop 0\n\tbuffer_load_dwordx4 %1, %2, %3 offen sc1 lds" :: "s"(ldsaddr), "v"(voff), "s"(srd), "s"(soff) : "memory");
}
DI void dma4(unsigned ldsaddr, unsigned voff, rsrc_t srd, unsigned soff) {
    asm volatile("s_mov_b32 m0, %0\n\ts_nop 0\n\tbuffer_load_dword %1, %2, %3 offen lds" :: "s"(ldsaddr), "v"(voff), "s"(srd), "s"(soff) : "memory");
}
DI void st16c(u32x4 v, rsrc_t srd, unsigned voff, unsigned soff) { __builtin_amdgcn_raw_buffer_store_b128(v, srd, voff + soff, 0, 16); }
DI bf16x8 lds128(unsigned a) { return *(const LAS bf16x8*)(size_t)a; }
DI s16x4 ldstr(unsigned a) { return __builtin_amdgcn_ds_read_tr16_b64_v4i16((LAS s16x4*)(size_t)a); }
DI bf16x8 cat8(s16x4 lo, s16x4 hi) { return __builtin_shufflevector(lo, hi, 0, 1, 2, 3, 4, 5, 6, 7); }
DI u32x4 pair16(f32x4 a, f32x4 b) {
    unsigned a0 = pk2(a[0], a[1]), a1 = pk2(a[2], a[3]), b0 = pk2(b[0], b[1]), b1 = pk2(b[2], b[3]);
    auto r0 = __builtin_amdgcn_permlane16_swap(a0, b0, false, false);
    auto r1 = __builtin_amdgcn_permlane16_swap(a1, b1, false, false);
    return (u32x4){r0[0], r1[0], r0[1], r1[1]};
}
DI float addf(float a, float b) { float r; asm("v_add_f32 %0, %1, %2" : "=v"(r) : "v"(a), "v"(b)); return r; }
template <int CTRL> DI float dpp_add(float v) { return v + __int_as_float(__builtin_amdgcn_update_dpp(0, __float_as_int(v), CTRL, 0xf, 0xf, true)); }
DI float wave_sum(float s) {
    s = dpp_add<0xB1>(s); s = dpp_add<0x4E>(s); s = dpp_add<0x141>(s); s = dpp_add<0x140>(s);
    auto r = __builtin_amdgcn_permlane16_swap(__float_as_uint(s), __float_as_uint(s), false, false); s = __uint_as_float(r[0]) + __uint_as_float(r[1]);
    r = __builtin_amdgcn_permlane32_swap(__float_as_uint(s), __float_as_uint(s), false, false); return __uint_as_float(r[0]) + __uint_as_float(r[1]);
}
template <int V> struct IC { static constexpr int value = V; };
template <int N, int I = 0, class Fn> DI void unroll_for(Fn&& f) { if constexpr (I < N) { f(IC<I>{}); unroll_for<N, I + 1>(f); } }

#define TOUCH_LEAD 8
#define RDSKIP 4
#define K1_PRIO(x) __builtin_amdgcn_s_setprio(x)
DI u32x4 pack8(f32x4 a, f32x4 b) { return (u32x4){pk2(a[0], a[1]), pk2(a[2], a[3]), pk2(b[0], b[1]), pk2(b[2], b[3])}; }
struct Ptrs { const u16 *x, *wqkv, *wo, *w1, *b1, *w2, *b2; u16* out; char* ws; };

typedef __attribute__((address_space(4))) const Ptrs* kptr_t;
DI Ptrs args_again() { kptr_t k = (kptr_t)__builtin_amdgcn_kernarg_segment_ptr(); PINS(k); Ptrs r; r.x = k->x; r.wqkv = k->wqkv; r.wo = k->wo; r.w1 = k->w1; r.b1 = k->b1; r.w2 = k->w2; r.b2 = k->b2; r.out = k->out; r.ws = k->ws; return r; }
DI int lane_again() { int l; asm volatile("v_mbcnt_lo_u32_b32 %0, -1, 0\n\tv_mbcnt_hi_u32_b32 %0, -1, %0" : "=v"(l)); return l; }
DI int bid_again() { int b = blockIdx.x; PINS(b); return b; }
DI int p2_tile(int b) { const int xcd = b & 7, idx = b >> 3; return (8 * ((xcd >> 1) & 1) + (idx & 7)) * 8 + 4 * (xcd >> 2) + (idx >> 3); }
DI rsrc_t px_rsrc(char* ws, int b, int other) { return mkrsrc(ws + OFF_PX + (size_t)(p2_tile(b) * 2 + ((b & 1) ^ other)) * 32768, 32768); }
DI void px_clear(int w, int lane) {
    const rsrc_t srdS = px_rsrc(args_again().ws, bid_again(), 0);
#pragma unroll
    for (int n = 0; n < 4; ++n) st16c((u32x4){~0u, ~0u, ~0u, ~0u}, srdS, lane * 16, w * 4096 + n * 1024);
}

constexpr int K1_ASTG = 32768, K1_BOFF = 3 * K1_ASTG, TAB_ROW = 157696, TAB_COL = TAB_ROW + 2048, TAB_B = TAB_COL + 1024;
template <int NF> DI int k1_pair(int y, int wr, int wc) {
    if (y == 3) return 6 + wc;
    const int t = y + (wr == 3 ? 0 : wr); return (NF == 7 ? 8 : 6) * wc + 2 * (t >= 3 ? t - 3 : t);
}
template <int NF>
DI void k1_mainloop(f32x4 (&acc)[4][NF], unsigned lds0, const Ptrs& P, unsigned soffA0, unsigned soffB0, int w, int lane, int pm, u64* flags, u64& tag, u64* st) {
    constexpr int PITCH = 64 * NF;
    constexpr int BSTG = 64 * PITCH;
    constexpr int LDB2 = (NF == 7) ? F_ * 2 : 3 * D_ * 2;
    constexpr int DUMMY = K1_BOFF + 2 * BSTG;
    const int wr = w >> 1, wc = w & 1, fr = lane & 15, fq = lane >> 4;
    const int myrow = blockIdx.x * 8 + w;
    u32x4 xr0, xr1; u64 epoch;
    asm volatile("global_load_dwordx4 %0, %3, %4\n\tglobal_load_dwordx4 %1, %3, %4 offset:1024\n\tglobal_load_dwordx2 %2, %5, %6 sc1"
                 : "=&v"(xr0), "=&v"(xr1), "=&v"(epoch) : "v"(lane * 16), "s"(P.x + (size_t)myrow * D_), "v"(0), "s"(flags) : "memory");
    const rsrc_t srdA = mkrsrc(P.x, S_ * D_ * 2), srdB = (NF == 7) ? mkrsrc(P.w1, D_ * F_ * 2) : mkrsrc(P.wqkv, D_ * 3 * D_ * 2);
    const unsigned voffA = (unsigned)((8 * w + (lane >> 3)) * 2048 + (((lane & 7) ^ (4 * (w & 1) + (lane >> 4))) << 4));
    const unsigned ldsA = lds0 + 1024u * w;
#pragma unroll
    for (int i = 0; i < 4; ++i) dma16(ldsA + i * 8192, voffA, srdA, soffA0 + i * (64 * 2048));
    unsigned voffB;
    {
        const int q = 64 * w + lane, row = q / (4 * NF), c = q % (4 * NF);
        voffB = (NF == 7) ? (unsigned)(row * LDB2 + (c << 4)) : (unsigned)(row * LDB2 + (c >> 3) * 2048 + (((c & 7) ^ (((row >> 1) & 1) << 2)) << 4));
        if (w >= NF) voffB = OOB;
    }
    const unsigned ldsB = (w < NF) ? lds0 + K1_BOFF + 1024u * w : lds0 + DUMMY;
    const unsigned bmul = (w < NF) ? 16u * PITCH : 0u;
#define K1_SETB(v) do { dma16(ldsB + bmul * 2 * (v), voffB, srdB, soffB0 + (v) * (32 * LDB2)); dma16(ldsB + bmul * (2 * (v) + 1), voffB, srdB, soffB0 + (v) * (32 * LDB2) + 16 * LDB2); } while (0)
#define K1_SETA(v) do { const unsigned la_ = ldsA + ((v) + 1) * 16384, sa_ = soffA0 + (((v) + 1) & 1) * (128 * 2048) + (((v) + 1) >> 1) * 128; \
        dma16(la_, voffA, srdA, sa_); dma16(la_ + 8192, voffA, srdA, sa_ + 64 * 2048); } while (0)
    K1_SETB(0); K1_SETB(1);
    SBAR();
    const unsigned ra0 = lds0 + (64 * wr + fr) * 128 + ((fq ^ (fr >> 1)) << 4), ra1 = ra0 ^ 64u;
    unsigned rb[4];
    {
        const int q = (lane & 15) >> 2, p = lane & 3;
        const unsigned rowp = lds0 + K1_BOFF + (8 * fq + q) * PITCH;
#pragma unroll
        for (int y = 0; y < 3; ++y) { const int f = k1_pair<NF>(y, wr, wc); rb[y] = rowp + 16 * p + ((NF == 7) ? 32 * f : ((32 * f) ^ ((q >> 1) << 6))); }
        rb[3] = rowp + 8 * p + 32 * k1_pair<NF>(3, wr, wc);
    }
    bf16x8 xf[2][4]; s16x4 wl[2][NF], wh[2][NF];
    unsigned ca, cb[4];
    f32x4 cs0 = {0.f, 0.f, 0.f, 0.f}, cs1 = {0.f, 0.f, 0.f, 0.f};
    bf16x8 allones = {0x3f80, 0x3f80, 0x3f80, 0x3f80, 0x3f80, 0x3f80, 0x3f80, 0x3f80}; PINV(allones);
    bf16x8 csel;
#define K1_ADDR(st_, slot, ks) do { ca = ((ks) ? ra1 : ra0) + (st_) * K1_ASTG; \
        _Pragma("unroll") for (int i = 0; i < (NF == 7 ? 4 : 3); ++i) cb[i] = rb[i] + (slot) * (32 * PITCH); \
        asm volatile("" : "+v"(ca), "+v"(cb[0]), "+v"(cb[1]), "+v"(cb[2])); if (NF == 7) PINV(cb[3]); } while (0)
#define K1_RD1(S, r) do { constexpr int n_ = ((r) < 12) ? (r) / 3 : 4 + ((r) - 12) / 2, k_ = ((r) < 12) ? (r) % 3 : ((r) - 12) % 2; \
        const unsigned b_ = cb[n_ >> 1] + 8 * (n_ & 1); \
        if (k_ == 0) wl[S][n_] = ldstr(b_); else if (k_ == 1) wh[S][n_] = ldstr(b_ + 4 * PITCH); else xf[S][n_ & 3] = lds128(ca + (n_ & 3) * 2048); } while (0)
#define K1_D(ADDR) do { \
        const unsigned la0_ = la, sa0_ = sa, lb0_ = lb, sb0_ = sb; \
        dma16(la0_, voffA, srdA, sa0_); SBAR(); \
        ADDR; \
        dma16(la0_ + 8192, voffA, srdA, sa0_ + 64 * 2048); SBAR(); \
        lb += 2 * bmul; lb = (lb == lbend) ? ldsB : lb; sb += 32 * LDB2; PINS(lb); PINS(sb); \
        dma16(lb0_, voffB, srdB, sb0_); SBAR(); \
        la += 16384; la = (la == laend) ? ldsA : la; ++v; sa = (v < 31) ? sa + da : OOB; da = 128 - da; PINS(la); PINS(sa); PINS(da); PINS(v); \
        dma16(lb0_ + bmul, voffB, srdB, sb0_ + 16 * LDB2); \
        WAITVL_BAR(8); } while (0)
#define CSMM(c, a) asm volatile("v_mfma_f32_16x16x32_bf16 %0, %1, %2, %0" : "+v"(c) : "v"(a), "v"(allones))
#define K1_CS(S, j) do { if constexpr (j == 3) { CSMM(cs1, cat8(wl[S][1], wh[S][1])); if (NF == 7) { csel = (wr == 3) ? cat8(wl[S][NF - 1], wh[S][NF - 1]) : cat8(wl[S][0], wh[S][0]); PINV(csel); } } \
        if constexpr (j == 7) CSMM(cs0, NF == 7 ? csel : cat8(wl[S][0], wh[S][0])); } while (0)
#define K1_M(S) do { SBAR(); K1_PRIO(1); \
        unroll_for<4 * NF>([&](auto J) { constexpr int j = J.value, n = j >> 2, m = (n & 1) ? 3 - (j & 3) : (j & 3), r = j - j / RDSKIP; \
            constexpr bool rd = j % RDSKIP != RDSKIP - 1 && r < 4 + 2 * NF; \
            acc[m][n] = MFMA16(cat8(wl[S][n], wh[S][n]), xf[S][m], acc[m][n]); \
            if constexpr (rd) K1_RD1((S) ^ 1, r); K1_CS(S, j); SBAR(); }); \
        K1_PRIO(0); WAITL0(); BAR(); SBAR(); } while (0)
    K1_ADDR(0, 0, 0);
#pragma unroll
    for (int m = 0; m < 4; ++m)
#pragma unroll
        for (int n = 0; n < NF; ++n) { acc[m][n] = (f32x4){0.f, 0.f, 0.f, 0.f}; PINV(acc[m][n]); }
    SBAR();
    K1_SETA(1); K1_SETB(2); K1_SETA(2);
    dma4((NF == 7 && w < 2) ? lds0 + TAB_B + 256 * w : lds0 + DUMMY, (NF == 7 && w < 2) ? (unsigned)(w * 256 + lane * 4) : OOB, mkrsrc(P.b1, F_ * 2), soffB0);
    {
        asm volatile("s_waitcnt vmcnt(15)" : "+v"(xr0), "+v"(xr1), "+v"(epoch));
        tag = (((u64)(unsigned)__builtin_amdgcn_readfirstlane((unsigned)(epoch >> 32)) << 32) | (unsigned)__builtin_amdgcn_readfirstlane((unsigned)epoch)) + 0x9E3779B97F4A7C15ull;
        f32x2 f[8];
#pragma unroll
        for (int i = 0; i < 4; ++i) { f[i] = (f32x2){blo(xr0[i]), bhi(xr0[i])}; f[4 + i] = (f32x2){blo(xr1[i]), bhi(xr1[i])}; }
        const f32x2 s = ((f[0] + f[1]) + (f[2] + f[3])) + ((f[4] + f[5]) + (f[6] + f[7]));
        const float mean = wave_sum(s[0] + s[1]) * (1.0f / D_);
        f32x2 q = {0.f, 0.f};
#pragma unroll
        for (int i = 0; i < 8; ++i) { const f32x2 d = f[i] - mean; q = __builtin_elementwise_fma(d, d, q); }
        const float sd = __builtin_amdgcn_sqrtf(rbf(rbf(wave_sum(q[0] + q[1]) * (1.0f / D_)) + 1e-5f));
        if (lane == 0) __hip_atomic_store((GAS u64*)(flags + FL_RS + myrow), (tag << 32) | pk2(mean, sd), RLX_AGENT);
    }
    STAMPC(0, 11);
    WAITV_BAR(10);
    STAMPC(0, 12);
    unroll_for<4 + 2 * NF>([&](auto J) { K1_RD1(0, J.value); });
    int v = 3;
    const unsigned lbend = ldsB + 8 * bmul, laend = ldsA + 3 * K1_ASTG;
    unsigned lb = ldsB + 6 * bmul, sb = soffB0 + 3 * 32 * LDB2, la = ldsA + 2 * K1_ASTG, sa = soffA0 + 2 * 128, da = 128u * 2048u;
    if (w >= 4) K1_D();
    STAMPC(0, 10);
    unsigned rs = 0, rq = 1;
    for (int t = 0; t < 14; ++t) {
        K1_D(K1_ADDR(rs, rq, 1); rs = (rs == 2) ? 0 : rs + 1; rq = (rq + 1) & 3; PINS(rs); PINS(rq));
        K1_M(0);
        K1_D(K1_ADDR(rs, rq, 0); rq = rq + 1; PINS(rq));
        K1_M(1);
    }
    K1_D(K1_ADDR(2, 1, 1));
    K1_M(0);
    K1_ADDR(0, 2, 0); WAITV_BAR(4);
    K1_M(1);
    const int lane2 = lane_again();
    const u64* const grp = flags + FL_RS + pm * 256 + (w & 3) * 64;
    u64 gr;
    K1_ADDR(0, 3, 1);
    asm volatile("s_waitcnt vmcnt(0)\n\tglobal_load_dwordx2 %0, %1, %2 sc1\n\ts_barrier" : "=v"(gr) : "v"(lane2 * 8), "s"(grp) : "memory");
    K1_M(0);
    BAR();
    K1_M(1);
    asm volatile("s_nop 7" : "+v"(cs0), "+v"(cs1));
    if ((lane2 & 15) == 0) {
        const int fq = lane2 >> 4;
        if (NF == 7 && wr == 3) *(LAS f32x4*)(size_t)(lds0 + TAB_COL + 64 * k1_pair<NF>(3, wr, wc) + 16 * fq) = cs0;
        else { const unsigned tc = lds0 + TAB_COL + 64 * k1_pair<NF>(0, wr, wc) + 32 * fq; *(LAS f32x4*)(size_t)tc = cs0; *(LAS f32x4*)(size_t)(tc + 16) = cs1; }
    }
    asm volatile("s_waitcnt vmcnt(0)" : "+v"(gr) :: "memory");
    if (w < 4) {
        for (unsigned spins = 0; spins < (1u << 20) && !__all((unsigned)(gr >> 32) == (unsigned)tag); ++spins) gr = __hip_atomic_load((GAS const u64*)(grp + lane2), RLX_AGENT);
        const float r = __builtin_amdgcn_rcpf(bhi((unsigned)gr));
        *(LAS f32x2*)(size_t)(lds0 + TAB_ROW + (w * 64 + lane2) * 8) = (f32x2){r, r * blo((unsigned)gr)};
        BAR();
    }
    WAITL0(); BAR();
#undef K1_ADDR
#undef K1_RD1
#undef K1_D
#undef K1_M
#undef K1_CS
#undef CSMM
#undef K1_SETA
#undef K1_SETB
}

DI f32x2 gelu2(f32x2 v) {
    const float c0 = -2.0f * 0.7978845608028654f * 1.4426950408889634f, c1 = c0 * 0.044715f;
    const f32x2 z = v * __builtin_elementwise_fma(v * v, (f32x2){c1, c1}, (f32x2){c0, c0});
    const f32x2 d = (f32x2){__builtin_amdgcn_exp2f(z[0]), __builtin_amdgcn_exp2f(z[1])} + 1.0f;
    return v * (f32x2){__builtin_amdgcn_rcpf(d[0]), __builtin_amdgcn_rcpf(d[1])};
}
DI f32x4 gelu4(f32x4 v) {
    const f32x2 lo = gelu2((f32x2){v[0], v[1]}), hi = gelu2((f32x2){v[2], v[3]});
    return (f32x4){lo[0], lo[1], hi[0], hi[1]};
}
DI f32x4 sp4(float v) { return (f32x4){v, v, v, v}; }
DI f32x4 fma4(f32x4 a, f32x4 b, f32x4 c) { return __builtin_elementwise_fma(a, b, c); }

DI int crow(int r, int hi) { return (r & 3) + 8 * (r >> 2) + 4 * hi; }
constexpr int AT_Q = 0, AT_K = 32768, AT_V = 98304;

template <int MODE> DI void at_mask(f32x16& s, int ql, int hi) {
    if (MODE == 0) return;
#pragma unroll
    for (int r = 0; r < 16; ++r) { const int k = crow(r, hi); const bool keep = (MODE == 1) ? (k <= ql) : (k > ql); s[r] = keep ? s[r] : -INFINITY; }
}
struct AtAddr { unsigned k0, k1, v0, v1; };
template <int MODE> DI float at_max_tile(const AtAddr& a, int kt, bf16x8 q0, bf16x8 q1, float mx, int ql, int hi) {
    f32x16 s = {};
    s = MFMA32(lds128(a.k0 + kt * 2048), q0, s); s = MFMA32(lds128(a.k1 + kt * 2048), q1, s);
    at_mask<MODE>(s, ql, hi);
#pragma unroll
    for (int r = 0; r < 16; r += 2) mx = __builtin_fmaxf(__builtin_fmaxf(mx, s[r]), s[r + 1]);
    return mx;
}
DI float xhalf_max(float m) { auto r = __builtin_amdgcn_permlane32_swap(__float_as_uint(m), __float_as_uint(m), false, false); return __builtin_fmaxf(__uint_as_float(r[0]), __uint_as_float(r[1])); }
DI f32x16 splat16(float v) { f32x16 r; _Pragma("unroll") for (int i = 0; i < 16; ++i) r[i] = v; return r; }
#define AT_LMAX 1.0e18f
struct AtUnit { bf16x8 q0, q1; f32x16 negm, o; f32x4 l; };
DI void at_exp_pack(f32x16 s, u32x4& p0, u32x4& p1) {
#pragma unroll
    for (int r = 0; r < 16; ++r) s[r] = __builtin_amdgcn_exp2f(s[r]);
    p0 = (u32x4){pk2(s[0], s[1]), pk2(s[2], s[3]), pk2(s[4], s[5]), pk2(s[6], s[7])};
    p1 = (u32x4){pk2(s[8], s[9]), pk2(s[10], s[11]), pk2(s[12], s[13]), pk2(s[14], s[15])};
}
DI f32x16 at_scores(const AtUnit& u, bf16x8 k0, bf16x8 k1) { return MFMA32(k1, u.q1, MFMA32(k0, u.q0, u.negm)); }
DI void at_accum(AtUnit& u, f32x16 s, bf16x8 v0, bf16x8 v1, bf16x8 ones) {
    u32x4 p0, p1; at_exp_pack(s, p0, p1);
    u.o = MFMA32(v0, BF8(p0), u.o); u.l = MFMA16(ones, BF8(p0), u.l);
    u.o = MFMA32(v1, BF8(p1), u.o); u.l = MFMA16(ones, BF8(p1), u.l);
}
DI bf16x8 at_v(const AtAddr& a, unsigned off) { return cat8(ldstr(a.v0 + off), ldstr(a.v1 + off)); }
DI void at_single(AtUnit& u, const AtAddr& a, int kt, bf16x8 ones) {
    const unsigned o = kt * 2048;
    const f32x16 s = at_scores(u, lds128(a.k0 + o), lds128(a.k1 + o));
    at_accum(u, s, at_v(a, o), at_v(a, o + 1024), ones);
}
#define AT_EXP4(s_, i) do { s_[i] = __builtin_amdgcn_exp2f(s_[i]); s_[i + 1] = __builtin_amdgcn_exp2f(s_[i + 1]); s_[i + 2] = __builtin_amdgcn_exp2f(s_[i + 2]); s_[i + 3] = __builtin_amdgcn_exp2f(s_[i + 3]); } while (0)
#define AT_G(MF, s_, i, p_) do { MF; AT_EXP4(s_, i); p_[((i) & 7) / 2] = pk2(s_[i], s_[i + 1]); p_[((i) & 7) / 2 + 1] = pk2(s_[i + 2], s_[i + 3]); PINV(p_); SBAR(); } while (0)
DI void at_body(AtUnit& x, AtUnit& y, const AtAddr& a, bf16x8 (&kf)[4], int kt, int ktn, bf16x8 ones) {
    unsigned va = a.v0 + kt * 2048, vb = a.v1 + kt * 2048, ka = a.k0 + ktn * 2048, kb = a.k1 + ktn * 2048;
    asm volatile("" : "+v"(va), "+v"(vb), "+v"(ka), "+v"(kb));
    SBAR();
    bf16x8 vf[4];
#pragma unroll
    for (int i = 0; i < 4; ++i) vf[i] = cat8(ldstr(va + i * 1024), ldstr(vb + i * 1024));
    f32x16 s0 = MFMA32(kf[0], x.q0, x.negm); s0 = MFMA32(kf[1], x.q1, s0);
    f32x16 s1 = MFMA32(kf[0], y.q0, y.negm), s2, s3; PINV(s0); PINV(s1); SBAR();
    u32x4 p0, p1, p2, p3;
    AT_G(s1 = MFMA32(kf[1], y.q1, s1); PINV(s1), s0, 0, p0);
    AT_G(s2 = MFMA32(kf[2], x.q0, x.negm); PINV(s2), s0, 4, p0);
    AT_G(s2 = MFMA32(kf[3], x.q1, s2); PINV(s2), s0, 8, p1);
    AT_G(s3 = MFMA32(kf[2], y.q0, y.negm); PINV(s3), s0, 12, p1);
    AT_G(s3 = MFMA32(kf[3], y.q1, s3); PINV(s3), s1, 0, p2);
    kf[0] = lds128(ka); kf[1] = lds128(kb); kf[2] = lds128(ka + 2048); kf[3] = lds128(kb + 2048);
    AT_G(x.o = MFMA32(vf[0], BF8(p0), x.o); PINV(x.o), s1, 4, p2);
    AT_G(x.o = MFMA32(vf[1], BF8(p1), x.o); PINV(x.o), s1, 8, p3);
    AT_G(x.l = MFMA16(ones, BF8(p0), x.l); x.l = MFMA16(ones, BF8(p1), x.l); PINV(x.l), s1, 12, p3);
    AT_G(y.o = MFMA32(vf[0], BF8(p2), y.o); PINV(y.o), s2, 0, p0);
    AT_G(y.o = MFMA32(vf[1], BF8(p3), y.o); PINV(y.o), s2, 4, p0);
    AT_G(y.l = MFMA16(ones, BF8(p2), y.l); y.l = MFMA16(ones, BF8(p3), y.l); PINV(y.l), s2, 8, p1);
    AT_G(x.o = MFMA32(vf[2], BF8(p0), x.o); PINV(x.o), s2, 12, p1);
    AT_G(x.l = MFMA16(ones, BF8(p0), x.l); PINV(x.l), s3, 0, p2);
    AT_G(x.o = MFMA32(vf[3], BF8(p1), x.o); PINV(x.o), s3, 4, p2);
    AT_G(x.l = MFMA16(ones, BF8(p1), x.l); y.o = MFMA32(vf[2], BF8(p2), y.o); PINV(x.l); PINV(y.o), s3, 8, p3);
    AT_G(y.l = MFMA16(ones, BF8(p2), y.l); PINV(y.l), s3, 12, p3);
    y.o = MFMA32(vf[3], BF8(p3), y.o); y.l = MFMA16(ones, BF8(p3), y.l);
    SBAR();
}
DI void at_kload(bf16x8 (&kf)[4], const AtAddr& a, int kt) { const unsigned ka = a.k0 + kt * 2048, kb = a.k1 + kt * 2048; kf[0] = lds128(ka); kf[1] = lds128(kb); kf[2] = lds128(ka + 2048); kf[3] = lds128(kb + 2048); }
DI void at_masks(u32x4& md0, u32x4& md1, int ql, int hi) {
#pragma unroll
    for (int r = 0; r < 4; ++r) {
        md0[r] = (crow(2 * r, hi) <= ql ? 0xffffu : 0u) | (crow(2 * r + 1, hi) <= ql ? 0xffff0000u : 0u);
        md1[r] = (crow(8 + 2 * r, hi) <= ql ? 0xffffu : 0u) | (crow(9 + 2 * r, hi) <= ql ? 0xffff0000u : 0u);
    }
}
template <bool FAR> DI void at_edges(AtUnit& u, const AtAddr& a, int t, bf16x8 ones, int ql, int hi) {
    const unsigned od = t * 2048, of = od + 16384;
    const bf16x8 kd0 = lds128(a.k0 + od), kd1 = lds128(a.k1 + od), vd0 = at_v(a, od), vd1 = at_v(a, od + 1024);
    bf16x8 kf0, kf1, vf0, vf1;
    if (FAR) { kf0 = lds128(a.k0 + of); kf1 = lds128(a.k1 + of); vf0 = at_v(a, of); vf1 = at_v(a, of + 1024); }
    SBAR();
    f32x16 s = at_scores(u, kd0, kd1);
    if (FAR) {
        const f32x16 sf = at_scores(u, kf0, kf1);
#pragma unroll
        for (int r = 0; r < 16; ++r) s[r] = (crow(r, hi) <= ql) ? s[r] : sf[r];
    } else at_mask<1>(s, ql, hi);
    u32x4 p0, p1; at_exp_pack(s, p0, p1);
    u.l = MFMA16(ones, BF8(p0), u.l); u.l = MFMA16(ones, BF8(p1), u.l);
    if (FAR) {
        u32x4 md0, md1; at_masks(md0, md1, ql, hi);
        const u32x4 d0 = p0 & md0, d1 = p1 & md1;
        u.o = MFMA32(vd0, BF8(d0), u.o); u.o = MFMA32(vd1, BF8(d1), u.o);
        u.o = MFMA32(vf0, BF8(p0 ^ d0), u.o); u.o = MFMA32(vf1, BF8(p1 ^ d1), u.o);
    } else { u.o = MFMA32(vd0, BF8(p0), u.o); u.o = MFMA32(vd1, BF8(p1), u.o); }
}
DI void at_edge_block(AtUnit& x, AtUnit& y, const AtAddr& a, const bf16x8 (&kp)[4], int t, bf16x8 ones, int ql, int hi) {
    bf16x8 kd[4], vd[4], vp[4];
    at_kload(kd, a, t);
    unsigned va = a.v0 + t * 2048, vb = a.v1 + t * 2048; PINV(va); PINV(vb);
    vp[2] = cat8(ldstr(va + 16384 + 2048), ldstr(vb + 16384 + 2048)); vp[3] = cat8(ldstr(va + 16384 + 3072), ldstr(vb + 16384 + 3072));
    SBAR();
    f32x16 sw = at_scores(x, kp[2], kp[3]);
    const f32x16 sxf = at_scores(x, kp[0], kp[1]); f32x16 sx = at_scores(x, kd[0], kd[1]);
    at_accum(x, sw, vp[2], vp[3], ones);
    SBAR();
    const f32x16 syf = at_scores(y, kp[2], kp[3]); f32x16 sy = at_scores(y, kd[2], kd[3]);
#pragma unroll
    for (int i = 0; i < 4; ++i) vd[i] = cat8(ldstr(va + i * 1024), ldstr(vb + i * 1024));
    vp[0] = cat8(ldstr(va + 16384), ldstr(vb + 16384)); vp[1] = cat8(ldstr(va + 16384 + 1024), ldstr(vb + 16384 + 1024));
#pragma unroll
    for (int r = 0; r < 16; ++r) sx[r] = (crow(r, hi) <= ql) ? sx[r] : sxf[r];
    u32x4 p0, p1, md0, md1; at_exp_pack(sx, p0, p1); at_masks(md0, md1, ql, hi);
    x.l = MFMA16(ones, BF8(p0), x.l); x.l = MFMA16(ones, BF8(p1), x.l);
    { const u32x4 d0 = p0 & md0, d1 = p1 & md1;
      x.o = MFMA32(vd[0], BF8(d0), x.o); x.o = MFMA32(vd[1], BF8(d1), x.o); x.o = MFMA32(vp[0], BF8(p0 ^ d0), x.o); x.o = MFMA32(vp[1], BF8(p1 ^ d1), x.o); }
#pragma unroll
    for (int r = 0; r < 16; ++r) sy[r] = (crow(r, hi) <= ql) ? sy[r] : syf[r];
    at_exp_pack(sy, p0, p1);
    y.l = MFMA16(ones, BF8(p0), y.l); y.l = MFMA16(ones, BF8(p1), y.l);
    { const u32x4 d0 = p0 & md0, d1 = p1 & md1;
      y.o = MFMA32(vd[2], BF8(d0), y.o); y.o = MFMA32(vd[3], BF8(d1), y.o); y.o = MFMA32(vp[2], BF8(p0 ^ d0), y.o); y.o = MFMA32(vp[3], BF8(p1 ^ d1), y.o); }
}
DI AtAddr at_addr(unsigned lds0, int hd, int lane) {
    const int ql = lane & 31, hi = lane >> 5, blk = (lane >> 4) & 1, q = (lane & 15) >> 2, p = lane & 3, c = 2 * blk + (p >> 1);
    AtAddr a; a.k0 = lds0 + AT_K + hd * 32768 + ql * 64 + ((hi ^ ((ql >> 2) & 3)) << 4); a.k1 = a.k0 ^ 32u;
    const unsigned vb = lds0 + AT_V + hd * 32768 + (4 * hi + q) * 64 + 8 * (p & 1);
    a.v0 = vb + ((c ^ hi) << 4); a.v1 = vb + 512 + ((c ^ (2 + hi)) << 4);
    asm volatile("" : "+v"(a.k0), "+v"(a.k1), "+v"(a.v0), "+v"(a.v1));
    return a;
}
DI void at_init(AtUnit& u, const AtAddr& a, unsigned qa, int t, int ql, int hi) {
    u.q0 = lds128(qa + t * 2048); u.q1 = lds128((qa ^ 32u) + t * 2048);
    const float m = xhalf_max(at_max_tile<1>(a, t, u.q0, u.q1, -INFINITY, ql, hi));
    u.negm = splat16(-m); u.o = splat16(0.f); u.l = (f32x4){0.f, 0.f, 0.f, 0.f};
}
DI void at_finish(AtUnit& u, unsigned lds0, int hd, int t, bool has_prev, bf16x8 ones, rsrc_t srdO, unsigned voff) {
    if (__builtin_expect(__any(!(u.l[0] < AT_LMAX)), 0)) {
        const int lane = lane_again(), ql = lane & 31, hi = lane >> 5; const AtAddr a = at_addr(lds0, hd, lane);
        float mx = -u.negm[0];
        for (int kt = 0; kt < t; ++kt) mx = at_max_tile<0>(a, kt, u.q0, u.q1, mx, ql, hi);
        if (has_prev) {
            mx = at_max_tile<2>(a, 8 + t, u.q0, u.q1, mx, ql, hi);
            for (int kt = 9 + t; kt < 16; ++kt) mx = at_max_tile<0>(a, kt, u.q0, u.q1, mx, ql, hi);
        }
        u.negm = splat16(-xhalf_max(mx)); u.o = splat16(0.f); u.l = (f32x4){0.f, 0.f, 0.f, 0.f};
        for (int kt = 0; kt < t; ++kt) at_single(u, a, kt, ones);
        if (has_prev) { at_edges<true>(u, a, t, ones, ql, hi); for (int kt = 9 + t; kt < 16; ++kt) at_single(u, a, kt, ones); }
        else at_edges<false>(u, a, t, ones, ql, hi);
    }
    const float rl = __builtin_amdgcn_rcpf(u.l[0]);
    u32x4 c[2];
#pragma unroll
    for (int k = 0; k < 2; ++k) {
        unsigned a0 = pk2(u.o[8 * k] * rl, u.o[8 * k + 1] * rl), a1 = pk2(u.o[8 * k + 2] * rl, u.o[8 * k + 3] * rl);
        unsigned b0 = pk2(u.o[8 * k + 4] * rl, u.o[8 * k + 5] * rl), b1 = pk2(u.o[8 * k + 6] * rl, u.o[8 * k + 7] * rl);
        auto r0 = __builtin_amdgcn_permlane32_swap(a0, b0, false, false);
        auto r1 = __builtin_amdgcn_permlane32_swap(a1, b1, false, false);
        c[k] = (u32x4){r0[0], r1[0], r0[1], r1[1]};
    }
#pragma unroll
    for (int i = 0; i < 4; ++i) { auto r = __builtin_amdgcn_permlane16_swap(c[0][i], c[1][i], false, false); c[0][i] = r[0]; c[1][i] = r[1]; }
    st16c(c[0], srdO, voff, (unsigned)(32 * t) * (KT_ * 2));
    st16c(c[1], srdO, voff, (unsigned)(32 * t + 16) * (KT_ * 2));
}

DI void wait_tags(const u64* p, bool need, u64 tag) {
    for (unsigned spins = 0; spins < (1u << 20); ++spins) {
        const u64 v = __hip_atomic_load((GAS const u64*)p, RLX_AGENT);
        if (__all(!need || v == tag)) break;
        __builtin_amdgcn_s_sleep(1);
    }
    __builtin_amdgcn_fence(__ATOMIC_ACQUIRE, "wavefront");
}

__global__ __launch_bounds__(512, 2) void fused_block(Ptrs P0) {
    extern __shared__ __attribute__((aligned(16))) char smem[];
    const unsigned lds0 = (unsigned)(size_t)(LAS char*)smem;
    const int w = __builtin_amdgcn_readfirstlane(threadIdx.x >> 6);
    u64* const st = (u64*)(P0.ws + OFF_ST);
    STAMP(0, 0); STAMPC(0, 8);
    u64 tag;
    {
        const Ptrs P = P0;
        const int xcd = blockIdx.x & 7, idx = blockIdx.x >> 3;
        u64* const flags = (u64*)(P.ws + OFF_FL);
        const int pm = 4 * (xcd & 1) + (idx & 3), j = 4 * (xcd >> 1) + ((idx >> 2) & 3);
        const int wr = w >> 1, wc = w & 1;
        if (idx >= 16) {
            f32x4 acc[4][7];
            k1_mainloop<7>(acc, lds0, P, (unsigned)(pm * 256 * 2048), (unsigned)(j * 448), w, lane_again(), pm, flags, tag, st);
            STAMP(0, 1); STAMPC(0, 9);
            const int lane = lane_again(), fr = lane & 15, fq = lane >> 4;
            int cy[4];
#pragma unroll
            for (int y = 0; y < 4; ++y) cy[y] = 16 * k1_pair<7>(y, wr, wc) + (y < 3 ? 8 : 4) * fq;
            px_clear(w, lane);
            const rsrc_t srdHO = mkrsrc(args_again().ws + OFF_HO, S_ * KT_ * 2);
            f32x4 bf[7], cs[7]; f32x2 rr[4];
#pragma unroll
            for (int n = 0; n < 7; ++n) {
                const u32x2 bb = *(const LAS u32x2*)(size_t)(lds0 + TAB_B + (cy[n >> 1] + 4 * (n & 1)) * 2);
                bf[n] = (f32x4){blo(bb[0]), bhi(bb[0]), blo(bb[1]), bhi(bb[1])}; cs[n] = *(const LAS f32x4*)(size_t)(lds0 + TAB_COL + (cy[n >> 1] + 4 * (n & 1)) * 4);
            }
#pragma unroll
            for (int m = 0; m < 4; ++m) rr[m] = *(const LAS f32x2*)(size_t)(lds0 + TAB_ROW + (64 * wr + 16 * m + fr) * 8);
#define HVAL(m, n) gelu4(fma4(acc[m][n], sp4(rr[m][0]), fma4(cs[n], sp4(-rr[m][1]), bf[n])))
            const unsigned vo = (unsigned)((pm * 256 + 64 * wr + fr) * KT_ + 224 * j) * 2u;
#pragma unroll
            for (int m = 0; m < 4; ++m) { const f32x4 hv = HVAL(m, 6); *(LAS u32x2*)(size_t)(lds0 + (64 * wr + 16 * m + fr) * 80 + 32 * wc + 8 * fq) = (u32x2){pk2(hv[0], hv[1]), pk2(hv[2], hv[3])}; }
            SBAR();
#pragma unroll
            for (int m = 0; m < 4; ++m) {
#pragma unroll
                for (int y = 0; y < 3; ++y) { st16c(pack8(HVAL(m, 2 * y), HVAL(m, 2 * y + 1)), srdHO, vo + cy[y] * 2, 16 * m * KT_ * 2); SBAR(); }
                if (m == 1) {
                    WAITL0(); BAR();
#pragma unroll
                    for (int i = 0; i < 2; ++i) st16c(*(const LAS u32x4*)(size_t)(lds0 + (64 * wr + 32 * wc + 16 * i + fr) * 80 + 16 * fq), srdHO, vo + 192 + 16 * fq, (32 * wc + 16 * i) * KT_ * 2);
                    SBAR();
                }
            }
            STAMP(0, 2);
        } else {
            f32x4 acc[4][6];
            k1_mainloop<6>(acc, lds0, P, (unsigned)(pm * 256 * 2048), (unsigned)(j * 128), w, lane_again(), pm, flags, tag, st);
            STAMP(0, 1); STAMPC(0, 9);
            const int lane = lane_again(), fr = lane & 15, fq = lane >> 4;
            {
                f32x2 rr[4];
#pragma unroll
                for (int m = 0; m < 4; ++m) rr[m] = *(const LAS f32x2*)(size_t)(lds0 + TAB_ROW + (64 * wr + 16 * m + fr) * 8);
                const rsrc_t srdX = mkrsrc(P.ws + OFF_KV + (size_t)(pm * 16 + j) * 65536, pm < 7 ? 65536 : 0);
                const unsigned rowoff = (unsigned)((64 * wr + fr) * 64 + ((fq ^ ((fr >> 2) & 3)) << 4));
#pragma unroll
                for (int y = 0; y < 3; ++y) {
                    const int blk = k1_pair<6>(y, wr, wc) >> 1;
                    const f32x4 c0 = *(const LAS f32x4*)(size_t)(lds0 + TAB_COL + (32 * blk + 8 * fq) * 4), c1 = *(const LAS f32x4*)(size_t)(lds0 + TAB_COL + (32 * blk + 8 * fq + 4) * 4);
                    const unsigned lb = lds0 + (blk < 2 ? AT_Q + blk * 16384 : AT_K + (blk - 2) * 32768) + rowoff;
                    const unsigned go = rowoff + (blk < 2 ? OOB : (unsigned)(blk - 2) * 16384u);
                    const float sc = blk < 2 ? 0.17677669529663687f * 1.4426950408889634f : 1.0f;
#pragma unroll
                    for (int m = 0; m < 4; ++m) {
                        const f32x4 r_ = sp4(rr[m][0] * sc), t_ = sp4(-rr[m][1] * sc);
                        const u32x4 v = pack8(fma4(acc[m][2 * y], r_, c0 * t_), fma4(acc[m][2 * y + 1], r_, c1 * t_));
                        st16c(v, srdX, go, m * 1024);
                        *(LAS u32x4*)(size_t)(lb + m * 1024) = v;
                    }
                }
            }
            WAITL0(); BAR();
            const int hd = w >> 2, uu = hd ? 7 - w : w, ta = 2 * uu, tb = ta + 1, ql = lane & 31, hi = lane >> 5;
            const AtAddr ad = at_addr(lds0, hd, lane);
            const short one = (((lane >> 4) & 1) == ((lane >> 2) & 1)) ? (short)0x3f80 : (short)0;
            const bf16x8 ones = {one, one, one, one, one, one, one, one};
            AtUnit ua, ub;
            at_init(ua, ad, ad.k0 - AT_K + AT_Q - hd * 16384, ta, ql, hi);
            at_init(ub, ad, ad.k0 - AT_K + AT_Q - hd * 16384, tb, ql, hi);
            at_single(ub, ad, ta, ones);
            bf16x8 kf[4];
            at_kload(kf, ad, 0);
            WAITV(0);
            if (lane == 0) __hip_atomic_store((GAS u64*)(flags + FL_KV + (pm * 16 + j) * 16 + w), tag, RLX_AGENT);
            STAMP(0, 2);
            for (int i = 0; i < uu; ++i) at_body(ua, ub, ad, kf, 2 * i, 2 * i + 2, ones);
            if (pm > 0) {
                if (uu < 2) {
                    const int lane = lane_again();
                    wait_tags(flags + FL_KV + ((pm - 1) * 16 + j) * 16 + (lane & 7), true, tag);
                    const rsrc_t srdX = mkrsrc(P.ws + OFF_KV + (size_t)((pm - 1) * 16 + j) * 65536, 65536);
                    const int rg = w & 3;
#pragma unroll
                    for (int q = 0; q < 16; ++q) dma16c(lds0 + AT_K + rg * 32768 + 16384 + q * 1024, lane * 16, srdX, rg * 16384 + q * 1024);
                }
                STAMP(0, 3);
                WAITVL_BAR(0);
                STAMP(0, 4);
            }
            unsigned junk0, junk1;
            {
                const Ptrs Q = args_again(); const unsigned l1 = (unsigned)lane_again(), qw = (unsigned)bid_again() * 8u + (unsigned)w;
                px_clear(w, l1);
                asm volatile("buffer_load_dword %0, %2, %3, 0 offen\n\tbuffer_load_dword %1, %4, %5, 0 offen" : "=&v"(junk0), "=&v"(junk1)
                             : "v"(l1 < 56u ? (qw * 56u + l1) * 128u : OOB), "s"(mkrsrc(Q.w2, F_ * D_ * 2)), "v"(l1 < 16u ? (qw * 16u + l1) * 128u : OOB), "s"(mkrsrc(Q.wo, D_ * D_ * 2)) : "memory");
            }
            if (pm > 0) {
                at_kload(kf, ad, 14);
                for (int i = 3; i > uu; --i) at_body(ua, ub, ad, kf, 8 + 2 * i, 6 + 2 * i, ones);
                const int l2 = lane_again();
                at_edge_block(ua, ub, ad, kf, ta, ones, l2 & 31, l2 >> 5);
            } else { const int l2 = lane_again(); const AtAddr a2 = at_addr(lds0, hd, l2); at_edges<false>(ua, a2, ta, ones, l2 & 31, l2 >> 5); at_edges<false>(ub, a2, tb, ones, l2 & 31, l2 >> 5); }
            const rsrc_t srdHO = mkrsrc(args_again().ws + OFF_HO, S_ * KT_ * 2);
            asm volatile("s_waitcnt vmcnt(0)" :: "v"(junk0), "v"(junk1) : "memory");
            const int l3 = lane_again();
            const unsigned vo = (unsigned)((pm * 256 + (l3 & 15)) * KT_ + F_ + 64 * j + 32 * hd + (l3 & 16) + 8 * (l3 >> 5)) * 2u;
            at_finish(ua, lds0, hd, ta, pm > 0, ones, srdHO, vo);
            at_finish(ub, lds0, hd, tb, pm > 0, ones, srdHO, vo);
            STAMP(0, 5);
        }
    }
    const Ptrs P = args_again();
    const int lane = lane_again(), fr = lane & 15, fq = lane >> 4;
    const int bid = bid_again(), xcd = bid & 7, idx = bid >> 3;
    u64* const flags = (u64*)(P.ws + OFF_FL);
    const int pm = 4 * (xcd & 1) + (idx & 3), j = 4 * (xcd >> 1) + ((idx >> 2) & 3);
    const int h = xcd & 1, rt = p2_tile(bid) >> 3, ct = p2_tile(bid) & 7;
    const rsrc_t srdHO = mkrsrc(P.ws + OFF_HO, S_ * KT_ * 2);
    const int g = w >> 2, w4 = w & 3, wr = w4 >> 1, wc = w4 & 1;
    const rsrc_t srdW2 = mkrsrc(P.w2, F_ * D_ * 2), srdWo = mkrsrc(P.wo, F_ * D_ * 2);
    const unsigned voffA = (unsigned)((8 * w4 + (lane >> 3)) * (KT_ * 2) + (((lane & 7) ^ (4 * (w4 & 1) + (lane >> 4))) << 4));
    const unsigned voffB = (unsigned)((4 * w4 + (lane >> 4)) * 2048 + (((lane & 15) ^ (((lane >> 4) | ((w4 >> 1) << 2)) << 1)) << 4));
    const unsigned soffA0 = (unsigned)(rt * 128 * (KT_ * 2) + h * 36 * 128), soffB0 = (unsigned)(ct * 256);
    const unsigned ldsW = lds0 + 1024u * w4;
    const int row = rt * 128 + 64 * wr + 16 * (2 * g + h) + fr, col = ct * 128 + 64 * wc;
    u32x2 xv[4], bv[4];
#pragma unroll
    for (int n = 0; n < 4; ++n) { xv[n] = *(const u32x2*)(P.x + (size_t)row * D_ + col + 16 * n + 4 * fq); bv[n] = *(const u32x2*)(P.b2 + col + 16 * n + 4 * fq); }
    WAITL0(); BAR();
    STAMP(1, 0); STAMPC(1, 8);
#define K2_B0(tau) do { _Pragma("unroll") for (int i = 0; i < 2; ++i) \
        dma16(ldsW + (tau) * 32768 + 16384 + (2 * g + i) * 4096, voffB, srdW2, soffB0 + (unsigned)((36 * h + (tau)) * 64 + 16 * (2 * g + i)) * 2048u); } while (0)
    K2_B0(0); K2_B0(1);
    WAITV_BAR(4);
    if (w == 0) {
        const int slot = (idx >= 16) ? j : 16 + j;
        if (lane == 0) __hip_atomic_store((GAS u64*)(flags + FL_P1 + pm * 32 + slot), tag, RLX_AGENT);
        STAMP(1, 5);
    }
    K2_B0(2); K2_B0(3); K2_B0(4);
    if (w == 0) {
        const int slot = (idx >= 16) ? j : 16 + j;
        const bool need = lane >= 32 || (h ? lane >= 10 : lane <= 10);
        wait_tags(flags + FL_P1 + (lane < 32 ? (rt >> 1) * 32 + lane : (pm ^ 4) * 32 + slot), need, tag);
    }
    const unsigned ra0 = lds0 + (64 * wr + fr) * 128 + ((fq ^ (fr >> 1)) << 4), ra1 = ra0 ^ 64u;
    unsigned rb[4];
    {
        const int q = (lane & 15) >> 2, p = lane & 3, s = q | ((fq & 1) << 2);
#pragma unroll
        for (int n = 0; n < 4; ++n) rb[n] = lds0 + 16384 + (8 * fq + q) * 256 + (((4 * wc + n) ^ s) << 5) + 8 * p;
    }
    f32x4 acc[4][4];
    bf16x8 xf[2][2][4]; s16x4 wl[2][2][4], wh[2][2][4];
    unsigned ca0, ca1, cb[4];
#define K2_ADDR(buf) do { const unsigned o_ = (buf) * 32768; ca0 = ra0 + o_; ca1 = ra1 + o_; _Pragma("unroll") for (int i = 0; i < 4; ++i) cb[i] = rb[i] + o_; \
        asm volatile("" : "+v"(ca0), "+v"(ca1), "+v"(cb[0]), "+v"(cb[1]), "+v"(cb[2]), "+v"(cb[3])); } while (0)
#define K2_RD1(S, r) do { constexpr int f_ = (r) / 3, k_ = f_ >> 2, n_ = f_ & 3; \
        if ((r) % 3 == 0) wl[S][k_][n_] = ldstr(cb[n_] + k_ * 8192); else if ((r) % 3 == 1) wh[S][k_][n_] = ldstr(cb[n_] + k_ * 8192 + 1024); \
        else xf[S][k_][n_] = lds128((k_ ? ca1 : ca0) + n_ * 2048); } while (0)
#define K2_MM(S) do { SBAR(); \
        unroll_for<32>([&](auto J) { constexpr int j = J.value, ks = j >> 4, n = (j >> 2) & 3, m = (n & 1) ? 3 - (j & 3) : (j & 3); \
            acc[m][n] = MFMA16(cat8(wl[S][ks][n], wh[S][ks][n]), xf[S][ks][m], acc[m][n]); \
            if constexpr (j < 24) K2_RD1((S) ^ 1, j); SBAR(); }); } while (0)
#define K2_STEP(S) do { \
        const unsigned l_ = lw, sa_ = sa, sb_ = sb; const rsrc_t rb_ = srdB; \
        dma16(l_ + 16384, voffB, rb_, sb_); \
        dma16c(l_, voffA, srdHO, sa_); \
        bc = (bc >= 3) ? bc - 3 : bc + 2; PINS(bc); K2_ADDR(bc); \
        dma16(l_ + 16384 + 4096, voffB, rb_, sb_ + 16 * 2048); \
        kt += 2; lw = ldsW + bc * 32768; sa = (kt < ktend) ? sa + 256 : OOB; PINS(kt); PINS(lw); PINS(sa); \
        dma16c(l_ + 4096, voffA, srdHO, sa_ + 32 * KT_ * 2); \
        sb = (kt < ktend) ? soffB0 + (unsigned)((kt >= 56 ? kt - 56 : kt) * (64 * 2048)) : OOB; srdB = (kt >= 56) ? srdWo : srdW2; PINS(sb); PINS(srdB); \
        dma16(l_ + 16384 + 8192, voffB, rb_, sb_ + 32 * 2048); \
        dma16c(l_ + 8192, voffA, srdHO, sa_ + 64 * KT_ * 2); \
        dma16(l_ + 16384 + 12288, voffB, rb_, sb_ + 48 * 2048); \
        dma16c(l_ + 12288, voffA, srdHO, sa_ + 96 * KT_ * 2); \
        BAR(); K2_MM(S); WAITVL_BAR(8); SBAR(); } while (0)
    K2_ADDR(g);
#pragma unroll
    for (int m = 0; m < 4; ++m)
#pragma unroll
        for (int n = 0; n < 4; ++n) { acc[m][n] = (f32x4){0.f, 0.f, 0.f, 0.f}; PINV(acc[m][n]); }
    BAR();
    STAMP(1, 6);
#pragma unroll
    for (int tau = 0; tau < 5; ++tau)
#pragma unroll
        for (int i = 0; i < 2; ++i) dma16c(ldsW + tau * 32768 + (2 * g + i) * 4096, voffA, srdHO, soffA0 + (unsigned)((2 * g + i) * (32 * KT_ * 2) + tau * 128));
    WAITV_BAR(6);
    unroll_for<24>([&](auto J) { K2_RD1(0, J.value); });
    WAITVL_BAR(4);
    if (g) WAITV_BAR(0);
    STAMPC(1, 10);
    int bc = g, kt = 36 * h + g + 5;
    const int ktend = 36 * h + 36;
    unsigned lw = ldsW + g * 32768, sa = soffA0 + (g + 5) * 128, sb = soffB0 + (unsigned)(kt * (64 * 2048));
    rsrc_t srdB = srdW2;
    for (int it = 0; it < 8; ++it) { K2_STEP(0); K2_STEP(1); }
    bc = (bc >= 3) ? bc - 3 : bc + 2; K2_ADDR(bc); BAR(); K2_MM(0); WAITVL_BAR(0); SBAR();
    BAR(); K2_MM(1); WAITL0(); BAR(); SBAR();
    if (!g) BAR();
    STAMP(1, 1); STAMPC(1, 9);
    f32x4 keep[4], send[4];
    {
        const unsigned wa = lds0 + w * 8192 + lane * 16;
#pragma unroll
        for (int n = 0; n < 4; ++n) {
            f32x4 a0 = acc[0][n], a1 = acc[1][n], a2 = acc[2][n], a3 = acc[3][n];
            asm volatile("" : "+v"(a0), "+v"(a1), "+v"(a2), "+v"(a3));
            *(LAS f32x4*)(size_t)(wa + n * 1024) = g ? a0 : a2;
            *(LAS f32x4*)(size_t)(wa + 4096 + n * 1024) = g ? a1 : a3;
            const f32x4 lo = g ? a2 : a0, hi = g ? a3 : a1;
            send[n] = h ? lo : hi; keep[n] = h ? hi : lo;
        }
    }
    WAITL0(); BAR();
    {
        const unsigned rbase = lds0 + (w ^ 4) * 8192 + lane * 16;
        const rsrc_t srdS = px_rsrc(P.ws, bid, 0);
#pragma unroll
        for (int n = 0; n < 4; ++n) {
            send[n] += *(const LAS f32x4*)(size_t)(rbase + (1 - h) * 4096 + n * 1024);
            st16c(__builtin_elementwise_min(__builtin_bit_cast(u32x4, send[n]), (u32x4){~1u, ~1u, ~1u, ~1u}), srdS, lane * 16, w * 4096 + n * 1024);
        }
#pragma unroll
        for (int n = 0; n < 4; ++n) {
            keep[n] += *(const LAS f32x4*)(size_t)(rbase + h * 4096 + n * 1024);
            keep[n] += (f32x4){blo(xv[n][0]) + blo(bv[n][0]), bhi(xv[n][0]) + bhi(bv[n][0]), blo(xv[n][1]) + blo(bv[n][1]), bhi(xv[n][1]) + bhi(bv[n][1])};
        }
    }
    STAMP(1, 2);
    {
        const rsrc_t srdR = px_rsrc(P.ws, bid, 1);
        u32x4 r[4];
        for (unsigned spins = 0; spins < (1u << 20); ++spins) {
            bool ok = true;
#pragma unroll
            for (int n = 0; n < 4; ++n) r[n] = __builtin_amdgcn_raw_buffer_load_b128(srdR, lane * 16 + w * 4096 + n * 1024, 0, 16);
#pragma unroll
            for (int n = 0; n < 4; ++n) ok = ok && r[n][0] != ~0u && r[n][1] != ~0u && r[n][2] != ~0u && r[n][3] != ~0u;
            if (__all(ok)) break;
            asm volatile("" ::: "memory");
        }
#pragma unroll
        for (int n = 0; n < 4; ++n) keep[n] += __builtin_bit_cast(f32x4, r[n]);
    }
    STAMP(1, 3);
    u16* op = P.out + (size_t)row * D_ + col + 16 * (fq & 1) + 8 * (fq >> 1);
    *(u32x4*)op = pair16(keep[0], keep[1]);
    *(u32x4*)(op + 32) = pair16(keep[2], keep[3]);
    if (blockIdx.x == 0 && w == 0) {
#pragma unroll
        for (int i = 0; i < 4; ++i) wait_tags(flags + FL_P1 + i * 64 + lane, true, tag);
        if (lane == 0) __hip_atomic_store((GAS u64*)flags, tag, RLX_AGENT);
    }
    STAMP(1, 4);
}

extern "C" void kernel_launch(void* const* d_in, const int* in_sizes, int n_in, void* d_out, int out_size, void* d_ws, size_t ws_size, hipStream_t stream) {
    Ptrs P;
    P.x = (const u16*)d_in[0]; P.wqkv = (const u16*)d_in[1]; P.wo = (const u16*)d_in[2];
    P.w1 = (const u16*)d_in[4]; P.b1 = (const u16*)d_in[5]; P.w2 = (const u16*)d_in[6]; P.b2 = (const u16*)d_in[7];
    P.out = (u16*)d_out; P.ws = (char*)d_ws + (ws_size - (size_t)(48u  __H0__ ));
    (void)hipFuncSetAttribute((const void*)fused_block, hipFuncAttributeMaxDynamicSharedMemorySize, LDS_BYTES);
    fused_block<<<256, 512, LDS_BYTES, stream>>>(P);
}
```

```cpp
#include <hip/hip_runtime.h>
#include <hip/hip_bf16.h>

typedef unsigned short u16;
typedef unsigned long long u64;
typedef short bf16x8 __attribute__((ext_vector_type(8)));
typedef short s16x4 __attribute__((ext_vector_type(4)));
typedef float f32x4 __attribute__((ext_vector_type(4)));
typedef float f32x16 __attribute__((ext_vector_type(16)));
typedef unsigned u32x4 __attribute__((ext_vector_type(4)));
typedef unsigned u32x2 __attribute__((ext_vector_type(2)));
typedef __bf16 bf2_t __attribute__((ext_vector_type(2)));
typedef float f32x2 __attribute__((ext_vector_type(2)));
#define LAS __attribute__((address_space(3)))
#define GAS __attribute__((address_space(1)))
#define DI __device__ __forceinline__
typedef __amdgpu_buffer_rsrc_t rsrc_t;

constexpr int S_ = 2048, D_ = 1024, F_ = 3584, KT_ = F_ + D_;
constexpr size_t OFF_HO = (size_t)S_ * D_ * 2;
constexpr size_t OFF_KV = OFF_HO + (size_t)S_ * KT_ * 2;
constexpr size_t OFF_PX = OFF_KV + (size_t)128 * 65536;
constexpr size_t OFF_FL = OFF_PX + (size_t)256 * 32768;
constexpr int FL_KV = 32, FL_P1 = FL_KV + 128 * 16, FL_RS = FL_P1 + 256;
constexpr size_t OFF_ST = OFF_FL + (size_t)384 * 128;
constexpr int LDS_BYTES = 163840;
constexpr unsigned OOB = 0x40000000u;

#define SBAR() __builtin_amdgcn_sched_barrier(0)
#define BAR() asm volatile("s_barrier" ::: "memory")
#define WAITV(n) asm volatile("s_waitcnt vmcnt(" #n ")" ::: "memory")
#define WAITL0() do { __builtin_amdgcn_s_waitcnt(0xC07F); asm volatile("" ::: "memory"); } while (0)
#define WAITVL_BAR(n) do { __builtin_amdgcn_s_waitcnt(0xC07F); asm volatile("s_waitcnt vmcnt(" #n ")\n\ts_barrier" ::: "memory"); } while (0)
#define WAITV_BAR(n) asm volatile("s_waitcnt vmcnt(" #n ")\n\ts_barrier" ::: "memory")
#define PINS(x) asm volatile("" : "+s"(x))
#define PINV(x) asm volatile("" : "+v"(x))
#define MFMA16(a, b, c) __builtin_amdgcn_mfma_f32_16x16x32_bf16(a, b, c, 0, 0, 0)
#define MFMA32(a, b, c) __builtin_amdgcn_mfma_f32_32x32x16_bf16(a, b, c, 0, 0, 0)
#define RLX_AGENT __ATOMIC_RELAXED, __HIP_MEMORY_SCOPE_AGENT
#define BF8(x) __builtin_bit_cast(bf16x8, x)

#define STAMP(k, i)
#define STAMPC(k, i)

DI float blo(unsigned v) { return __uint_as_float(v << 16); }
DI float bhi(unsigned v) { return __uint_as_float(v & 0xffff0000u); }
DI unsigned pk2(float lo, float hi) { f32x2 v = {lo, hi}; return __builtin_bit_cast(unsigned, __builtin_convertvector(v, bf2_t)); }
DI float rbf(float f) { return blo(pk2(f, 0.f)); }
DI rsrc_t mkrsrc(const void* p, unsigned bytes) { return __builtin_amdgcn_make_buffer_rsrc((void*)p, (short)0, (int)bytes, 0x00020000); }
DI void dma16(unsigned ldsaddr, unsigned voff, rsrc_t srd, unsigned soff) {
    asm volatile("s_mov_b32 m0, %0\n\ts_nop 0\n\tbuffer_load_dwordx4 %1, %2, %3 offen lds" :: "s"(ldsaddr), "v"(voff), "s"(srd), "s"(soff) : "memory");
}
DI void dma16c(unsigned ldsaddr, unsigned voff, rsrc_t srd, unsigned soff) {
    asm volatile("s_mov_b32 m0, %0\n\ts_nop 0\n\tbuffer_load_dwordx4 %1, %2, %3 offen sc1 lds" :: "s"(ldsaddr), "v"(voff), "s"(srd), "s"(soff) : "memory");
}
DI void dma4(unsigned ldsaddr, unsigned voff, rsrc_t srd, unsigned soff) {
    asm volatile("s_mov_b32 m0, %0\n\ts_nop 0\n\tbuffer_load_dword %1, %2, %3 offen lds" :: "s"(ldsaddr), "v"(voff), "s"(srd), "s"(soff) : "memory");
}
DI void st16c(u32x4 v, rsrc_t srd, unsigned voff, unsigned soff) { __builtin_amdgcn_raw_buffer_store_b128(v, srd, voff + soff, 0, 16); }
DI bf16x8 lds128(unsigned a) { return *(const LAS bf16x8*)(size_t)a; }
DI s16x4 ldstr(unsigned a) { return __builtin_amdgcn_ds_read_tr16_b64_v4i16((LAS s16x4*)(size_t)a); }
DI bf16x8 cat8(s16x4 lo, s16x4 hi) { return __builtin_shufflevector(lo, hi, 0, 1, 2, 3, 4, 5, 6, 7); }
DI u32x4 pair16(f32x4 a, f32x4 b) {
    unsigned a0 = pk2(a[0], a[1]), a1 = pk2(a[2], a[3]), b0 = pk2(b[0], b[1]), b1 = pk2(b[2], b[3]);
    auto r0 = __builtin_amdgcn_permlane16_swap(a0, b0, false, false);
    auto r1 = __builtin_amdgcn_permlane16_swap(a1, b1, false, false);
    return (u32x4){r0[0], r1[0], r0[1], r1[1]};
}
DI float dot2(unsigned a, unsigned b, float c) { return __builtin_amdgcn_fdot2_f32_bf16(__builtin_bit_cast(bf2_t, a), __builtin_bit_cast(bf2_t, b), c, false); }
DI float addf(float a, float b) { float r; asm("v_add_f32 %0, %1, %2" : "=v"(r) : "v"(a), "v"(b)); return r; }
template <int CTRL> DI float dpp_add(float v) { return v + __int_as_float(__builtin_amdgcn_update_dpp(0, __float_as_int(v), CTRL, 0xf, 0xf, true)); }
DI float half_sum(float s) {
    s = dpp_add<0xB1>(s); s = dpp_add<0x4E>(s); s = dpp_add<0x141>(s); s = dpp_add<0x140>(s);
    auto r = __builtin_amdgcn_permlane16_swap(__float_as_uint(s), __float_as_uint(s), false, false); return __uint_as_float(r[0]) + __uint_as_float(r[1]);
}
template <int V> struct IC { static constexpr int value = V; };
template <int N, int I = 0, class Fn> DI void unroll_for(Fn&& f) { if constexpr (I < N) { f(IC<I>{}); unroll_for<N, I + 1>(f); } }

#define TOUCH_LEAD 8
#define RDSKIP 4
#define K1_PRIO(x) __builtin_amdgcn_s_setprio(x)
DI u32x4 pack8(f32x4 a, f32x4 b) { return (u32x4){pk2(a[0], a[1]), pk2(a[2], a[3]), pk2(b[0], b[1]), pk2(b[2], b[3])}; }
struct Ptrs { const u16 *x, *wqkv, *wo, *w1, *b1, *w2, *b2; u16* out; char* ws; };

typedef __attribute__((address_space(4))) const Ptrs* kptr_t;
DI Ptrs args_again() { kptr_t k = (kptr_t)__builtin_amdgcn_kernarg_segment_ptr(); PINS(k); Ptrs r; r.x = k->x; r.wqkv = k->wqkv; r.wo = k->wo; r.w1 = k->w1; r.b1 = k->b1; r.w2 = k->w2; r.b2 = k->b2; r.out = k->out; r.ws = k->ws; return r; }
DI int lane_again() { int l; asm volatile("v_mbcnt_lo_u32_b32 %0, -1, 0\n\tv_mbcnt_hi_u32_b32 %0, -1, %0" : "=v"(l)); return l; }
DI int bid_again() { int b = blockIdx.x; PINS(b); return b; }
DI int p2_tile(int b) { const int xcd = b & 7, idx = b >> 3; return (8 * ((xcd >> 1) & 1) + (idx & 7)) * 8 + 4 * (xcd >> 2) + (idx >> 3); }
DI rsrc_t px_rsrc(char* ws, int b, int other) { return mkrsrc(ws + OFF_PX + (size_t)(p2_tile(b) * 2 + ((b & 1) ^ other)) * 32768, 32768); }
DI void px_clear(int w, int lane) {
    const rsrc_t srdS = px_rsrc(args_again().ws, bid_again(), 0);
#pragma unroll
    for (int n = 0; n < 4; ++n) st16c((u32x4){~0u, ~0u, ~0u, ~0u}, srdS, lane * 16, w * 4096 + n * 1024);
}

#define TNTAB 0xA864211Bu
#define TJ1 1
#define TJ2 1
#define LEADT 4
#define LOOK 6
constexpr int K1_ASTG = 32768, K1_BOFF = 3 * K1_ASTG, TAB_ROW = 157696, TAB_COL = TAB_ROW + 2048, TAB_B = TAB_COL + 1024;
template <int NF> DI int k1_pair(int y, int wr, int wc) {
    if (y == 3) return 6 + wc;
    const int t = y + (wr == 3 ? 0 : wr); return (NF == 7 ? 8 : 6) * wc + 2 * (t >= 3 ? t - 3 : t);
}
template <int NF>
DI void k1_mainloop(f32x4 (&acc)[4][NF], unsigned lds0, const Ptrs& P, unsigned soffA0, unsigned soffB0, int w, int lane, int pm, u64* flags, u64& tag, u64* st) {
    constexpr int PITCH = 64 * NF;
    constexpr int BSTG = 64 * PITCH;
    constexpr int LDB2 = (NF == 7) ? F_ * 2 : 3 * D_ * 2;
    constexpr int DUMMY = K1_BOFF + 2 * BSTG;
    const int wr = w >> 1, wc = w & 1, fr = lane & 15, fq = lane >> 4;
    const rsrc_t srdA = mkrsrc(P.x, S_ * D_ * 2), srdB = (NF == 7) ? mkrsrc(P.w1, D_ * F_ * 2) : mkrsrc(P.wqkv, D_ * 3 * D_ * 2);
    const unsigned voffA = (unsigned)((8 * w + (lane >> 3)) * 2048 + (((lane & 7) ^ (4 * (w & 1) + (lane >> 4))) << 4));
    const unsigned ldsA = lds0 + 1024u * w;
#pragma unroll
    for (int i = 0; i < 4; ++i) dma16(ldsA + i * 8192, voffA, srdA, soffA0 + i * (64 * 2048));
    unsigned voffB;
    {
        const unsigned q = 64 * w + lane, row = (q * (NF == 7 ? 2341u : 2731u)) >> 16, c = q - row * (4 * NF);
        voffB = (NF == 7) ? row * LDB2 + (c << 4) : row * LDB2 + (c >> 3) * 2048 + (((c & 7) ^ (((row >> 1) & 1) << 2)) << 4);
        if (w >= NF) voffB = OOB;
    }
    const unsigned ldsB = (w < NF) ? lds0 + K1_BOFF + 1024u * w : lds0 + DUMMY;
    const unsigned bmul = (w < NF) ? 16u * PITCH : 0u;
#define K1_SETB(v) do { dma16(ldsB + bmul * 2 * (v), voffB, srdB, soffB0 + (v) * (32 * LDB2)); dma16(ldsB + bmul * (2 * (v) + 1), voffB, srdB, soffB0 + (v) * (32 * LDB2) + 16 * LDB2); } while (0)
#define K1_SETA(v) do { const unsigned la_ = ldsA + ((v) + 1) * 16384, sa_ = soffA0 + (((v) + 1) & 1) * (128 * 2048) + (((v) + 1) >> 1) * 128; \
        dma16(la_, voffA, srdA, sa_); dma16(la_ + 8192, voffA, srdA, sa_ + 64 * 2048); } while (0)
    K1_SETB(0); K1_SETB(1);
    SBAR();
    const unsigned ra0 = lds0 + (64 * wr + fr) * 128 + ((fq ^ (fr >> 1)) << 4), ra1 = ra0 ^ 64u;
    unsigned rb[4];
    {
        const int q = (lane & 15) >> 2, p = lane & 3;
        const unsigned rowp = lds0 + K1_BOFF + (8 * fq + q) * PITCH;
#pragma unroll
        for (int y = 0; y < 3; ++y) { const int f = k1_pair<NF>(y, wr, wc); rb[y] = rowp + 16 * p + ((NF == 7) ? 32 * f : ((32 * f) ^ ((q >> 1) << 6))); }
        rb[3] = rowp + 8 * p + 32 * k1_pair<NF>(3, wr, wc);
    }
    bf16x8 xf[2][4]; s16x4 wl[2][NF], wh[2][NF];
    unsigned ca, cb[4];
    f32x4 cs0 = {0.f, 0.f, 0.f, 0.f}, cs1 = {0.f, 0.f, 0.f, 0.f};
    bf16x8 allones = {0x3f80, 0x3f80, 0x3f80, 0x3f80, 0x3f80, 0x3f80, 0x3f80, 0x3f80}; PINV(allones);
    bf16x8 csel;
#define K1_ADDR(st_, slot, ks) do { ca = ((ks) ? ra1 : ra0) + (st_) * K1_ASTG; \
        _Pragma("unroll") for (int i = 0; i < (NF == 7 ? 4 : 3); ++i) cb[i] = rb[i] + (slot) * (32 * PITCH); \
        asm volatile("" : "+v"(ca), "+v"(cb[0]), "+v"(cb[1]), "+v"(cb[2])); if (NF == 7) PINV(cb[3]); } while (0)
#define K1_RD1(S, r) do { constexpr int n_ = ((r) < 12) ? (r) / 3 : 4 + ((r) - 12) / 2, k_ = ((r) < 12) ? (r) % 3 : ((r) - 12) % 2; \
        const unsigned b_ = cb[n_ >> 1] + 8 * (n_ & 1); \
        if (k_ == 0) wl[S][n_] = ldstr(b_); else if (k_ == 1) wh[S][n_] = ldstr(b_ + 4 * PITCH); else xf[S][n_ & 3] = lds128(ca + (n_ & 3) * 2048); } while (0)
#define K1_DI(ADDR) do { \
        const unsigned la0_ = la, sa0_ = sa, lb0_ = lb, sb0_ = sb; \
        dma16(la0_, voffA, srdA, sa0_); SBAR(); \
        ADDR; \
        dma16(la0_ + 8192, voffA, srdA, sa0_ + 64 * 2048); SBAR(); \
        lb += 2 * bmul; lb = (lb == lbend) ? ldsB : lb; sb += 32 * LDB2; PINS(lb); PINS(sb); \
        dma16(lb0_, voffB, srdB, sb0_); SBAR(); \
        la += 16384; la = (la == laend) ? ldsA : la; ++v; sa = (v < 31) ? sa + da : OOB; da = 128 - da; PINS(la); PINS(sa); PINS(da); PINS(v); \
        dma16(lb0_ + bmul, voffB, srdB, sb0_ + 16 * LDB2); } while (0)
#define K1_D(ADDR) do { K1_DI(ADDR); WAITV_BAR(8); } while (0)
#define CSMM(c, a) asm volatile("v_mfma_f32_16x16x32_bf16 %0, %1, %2, %0" : "+v"(c) : "v"(a), "v"(allones))
#define K1_CS(S, j) do { if constexpr (j == 3) { CSMM(cs1, cat8(wl[S][1], wh[S][1])); if (NF == 7) { csel = (wr == 3) ? cat8(wl[S][NF - 1], wh[S][NF - 1]) : cat8(wl[S][0], wh[S][0]); PINV(csel); } } \
        if constexpr (j == 7) CSMM(cs0, NF == 7 ? csel : cat8(wl[S][0], wh[S][0])); } while (0)
#define K1_M(S) do { SBAR(); K1_PRIO(1); \
        unroll_for<4 * NF>([&](auto J) { constexpr int j = J.value, n = j >> 2, m = (n & 1) ? 3 - (j & 3) : (j & 3), r = j - j / RDSKIP; \
            constexpr bool rd = j % RDSKIP != RDSKIP - 1 && r < 4 + 2 * NF; \
            acc[m][n] = MFMA16(cat8(wl[S][n], wh[S][n]), xf[S][m], acc[m][n]); \
            if constexpr (rd) K1_RD1((S) ^ 1, r); K1_CS(S, j); SBAR(); }); \
        K1_PRIO(0); WAITL0(); BAR(); SBAR(); } while (0)
    K1_ADDR(0, 0, 0);
#pragma unroll
    for (int m = 0; m < 4; ++m)
#pragma unroll
        for (int n = 0; n < NF; ++n) { acc[m][n] = (f32x4){0.f, 0.f, 0.f, 0.f}; PINV(acc[m][n]); }
    SBAR();
    K1_SETA(1); K1_SETA(2); K1_SETB(2);
    int v = 3;
    const unsigned lbend = ldsB + 8 * bmul, laend = ldsA + 3 * K1_ASTG;
    unsigned lb = ldsB + 6 * bmul, sb = soffB0 + 3 * 32 * LDB2, la = ldsA + 2 * K1_ASTG, sa = soffA0 + 2 * 128, da = 128u * 2048u;
    STAMPC(0, 11);
    WAITV_BAR(6);
    STAMPC(0, 12);
    unroll_for<4 + 2 * NF>([&](auto J) { K1_RD1(0, J.value); });
    u64 epoch;
    asm volatile("global_load_dwordx2 %0, %1, %2 sc1" : "=v"(epoch) : "v"(0), "s"(flags) : "memory");
    if (NF == 7) dma4(w < 2 ? lds0 + TAB_B + 256 * w : lds0 + DUMMY, w < 2 ? (unsigned)(w * 256 + lane * 4) : OOB, mkrsrc(P.b1, F_ * 2), soffB0);
    K1_DI();
    K1_ADDR(0, 1, 1);
    WAITL0();
    STAMPC(0, 10);
    const int myrow = blockIdx.x * 8 + 2 * (w & 3);
    u32x4 xr[4];
    if (w >= 4) {
        BAR();
        asm volatile("global_load_dwordx4 %0, %4, %5\n\tglobal_load_dwordx4 %1, %4, %5 offset:1024\n\tglobal_load_dwordx4 %2, %4, %5 offset:2048\n\tglobal_load_dwordx4 %3, %4, %5 offset:3072"
                     : "=&v"(xr[0]), "=&v"(xr[1]), "=&v"(xr[2]), "=&v"(xr[3]) : "v"(lane * 16), "s"(P.x + (size_t)myrow * D_) : "memory");
        K1_DI();
    } else asm volatile("" : "=v"(xr[0]), "=v"(xr[1]), "=v"(xr[2]), "=v"(xr[3]));
    unsigned rs = 1, rq = 2;
    int tn = (TNTAB >> (4 * pm)) & 15;
#pragma nounroll
    for (int pass = 0; pass < 2; ++pass) {
#pragma nounroll
        for (int t = 0; t < tn; ++t) {
            WAITV_BAR(8);
            K1_M(0);
            K1_D(K1_ADDR(rs, rq, 0); rq = rq + 1; PINS(rq));
            K1_M(1);
            K1_DI(K1_ADDR(rs, rq, 1); rs = (rs == 2) ? 0 : rs + 1; rq = (rq + 1) & 3; PINS(rs); PINS(rq));
        }
        if (pass == 0) {
            asm volatile("" : "+v"(xr[0]), "+v"(xr[1]), "+v"(xr[2]), "+v"(xr[3]), "+v"(epoch));
            const int l1 = lane_again(); const Ptrs Q = args_again(); const unsigned qw = (unsigned)bid_again() * 4u + (unsigned)(w & 3);
            if (TJ1) dma4(lds0 + DUMMY, (NF == 7 || (l1 & 3) < 3) ? (unsigned)((16 * w + (l1 >> 2)) * LDB2 + (l1 & 3) * (NF == 7 ? 128 : 2048)) : OOB, srdB, soffB0 + (unsigned)(pm * 128 * LDB2));
            tag = (((u64)(unsigned)__builtin_amdgcn_readfirstlane((unsigned)(epoch >> 32)) << 32) | (unsigned)__builtin_amdgcn_readfirstlane((unsigned)epoch)) + 0x9E3779B97F4A7C15ull;
            if (w >= 4) {
                float s0 = 0.f, s1 = 0.f, q0 = 0.f, q1 = 0.f;
#pragma unroll
                for (int i = 0; i < 8; ++i) { const unsigned a_ = xr[i >> 2][i & 3], b_ = xr[2 + (i >> 2)][i & 3];
                    s0 = dot2(a_, 0x3f803f80u, s0); q0 = dot2(a_, a_, q0); s1 = dot2(b_, 0x3f803f80u, s1); q1 = dot2(b_, b_, q1); }
                auto rs_ = __builtin_amdgcn_permlane32_swap(__float_as_uint(s0), __float_as_uint(s1), false, false);
                auto rq_ = __builtin_amdgcn_permlane32_swap(__float_as_uint(q0), __float_as_uint(q1), false, false);
                const float mean = half_sum(__uint_as_float(rs_[0]) + __uint_as_float(rs_[1])) * (1.0f / D_);
                const float var = __builtin_fmaxf(__builtin_fmaf(-mean, mean, half_sum(__uint_as_float(rq_[0]) + __uint_as_float(rq_[1])) * (1.0f / D_)), 0.f);
                const float sd = __builtin_amdgcn_sqrtf(rbf(rbf(var) + 1e-5f));
                if ((l1 & 31) == 0) __hip_atomic_store((GAS u64*)(flags + FL_RS + myrow + (l1 >> 5)), (tag << 32) | pk2(mean, sd), RLX_AGENT);
            }
            if (TJ2) dma4(lds0 + DUMMY, (w < 4) ? (l1 < 56 ? (qw * 56u + l1) * 128u : OOB) : (l1 < 16 ? (qw * 16u + l1) * 128u : OOB), (w < 4) ? mkrsrc(Q.w2, F_ * D_ * 2) : mkrsrc(Q.wo, D_ * D_ * 2), 0);
            tn = 14 - tn;
        }
    }
    WAITV_BAR(8);
    K1_M(0);
    K1_ADDR(0, 2, 0); WAITV_BAR(4);
    K1_M(1);
    const int lane2 = lane_again();
    const u64* const grp = flags + FL_RS + pm * 256 + (w & 3) * 64;
    u64 gr;
    K1_ADDR(0, 3, 1);
    asm volatile("s_waitcnt vmcnt(0)\n\tglobal_load_dwordx2 %0, %1, %2 sc1" : "=v"(gr) : "v"(lane2 * 8), "s"(grp) : "memory");
    BAR();
    K1_M(0);
    px_clear(w, lane2);
    BAR();
    K1_M(1);
    asm volatile("s_nop 7" : "+v"(cs0), "+v"(cs1));
    if ((lane2 & 15) == 0) {
        const int fq = lane2 >> 4;
        if (NF == 7 && wr == 3) *(LAS f32x4*)(size_t)(lds0 + TAB_COL + 64 * k1_pair<NF>(3, wr, wc) + 16 * fq) = cs0;
        else { const unsigned tc = lds0 + TAB_COL + 64 * k1_pair<NF>(0, wr, wc) + 32 * fq; *(LAS f32x4*)(size_t)tc = cs0; *(LAS f32x4*)(size_t)(tc + 16) = cs1; }
    }
    asm volatile("s_waitcnt vmcnt(4)" : "+v"(gr) :: "memory");
    if (w < 4) {
        if (__builtin_expect(!__all((unsigned)(gr >> 32) == (unsigned)tag), 0)) {
            for (unsigned spins = 0; spins < (1u << 20); ++spins) {
                asm volatile("global_load_dwordx2 %0, %1, %2 sc1\n\ts_waitcnt vmcnt(0)" : "=&v"(gr) : "v"(lane2 * 8), "s"(grp) : "memory");
                if (__all((unsigned)(gr >> 32) == (unsigned)tag)) break;
            }
        }
        const float r = __builtin_amdgcn_rcpf(bhi((unsigned)gr));
        *(LAS f32x2*)(size_t)(lds0 + TAB_ROW + (w * 64 + lane2) * 8) = (f32x2){r, r * blo((unsigned)gr)};
        BAR();
    }
    WAITL0(); BAR();
#undef K1_ADDR
#undef K1_RD1
#undef K1_D
#undef K1_DI
#undef K1_M
#undef K1_CS
#undef CSMM
#undef K1_SETA
#undef K1_SETB
}

DI f32x2 gelu2(f32x2 v) {
    const float c0 = -2.0f * 0.7978845608028654f * 1.4426950408889634f, c1 = c0 * 0.044715f;
    const f32x2 z = v * __builtin_elementwise_fma(v * v, (f32x2){c1, c1}, (f32x2){c0, c0});
    const f32x2 d = (f32x2){__builtin_amdgcn_exp2f(z[0]), __builtin_amdgcn_exp2f(z[1])} + 1.0f;
    return v * (f32x2){__builtin_amdgcn_rcpf(d[0]), __builtin_amdgcn_rcpf(d[1])};
}
DI f32x4 gelu4(f32x4 v) {
    const f32x2 lo = gelu2((f32x2){v[0], v[1]}), hi = gelu2((f32x2){v[2], v[3]});
    return (f32x4){lo[0], lo[1], hi[0], hi[1]};
}
DI f32x4 sp4(float v) { return (f32x4){v, v, v, v}; }
DI f32x4 fma4(f32x4 a, f32x4 b, f32x4 c) { return __builtin_elementwise_fma(a, b, c); }

DI int crow(int r, int hi) { return (r & 3) + 8 * (r >> 2) + 4 * hi; }
constexpr int AT_Q = 0, AT_K = 32768, AT_V = 98304;

template <int MODE> DI void at_mask(f32x16& s, int ql, int hi) {
    if (MODE == 0) return;
#pragma unroll
    for (int r = 0; r < 16; ++r) { const int k = crow(r, hi); const bool keep = (MODE == 1) ? (k <= ql) : (k > ql); s[r] = keep ? s[r] : -INFINITY; }
}
struct AtAddr { unsigned k0, k1, v0, v1; };
template <int MODE> DI float at_max_tile(const AtAddr& a, int kt, bf16x8 q0, bf16x8 q1, float mx, int ql, int hi) {
    f32x16 s = {};
    s = MFMA32(lds128(a.k0 + kt * 2048), q0, s); s = MFMA32(lds128(a.k1 + kt * 2048), q1, s);
    at_mask<MODE>(s, ql, hi);
#pragma unroll
    for (int r = 0; r < 16; r += 2) mx = __builtin_fmaxf(__builtin_fmaxf(mx, s[r]), s[r + 1]);
    return mx;
}
DI float xhalf_max(float m) { auto r = __builtin_amdgcn_permlane32_swap(__float_as_uint(m), __float_as_uint(m), false, false); return __builtin_fmaxf(__uint_as_float(r[0]), __uint_as_float(r[1])); }
DI f32x16 splat16(float v) { f32x16 r; _Pragma("unroll") for (int i = 0; i < 16; ++i) r[i] = v; return r; }
#define AT_LMAX 1.0e18f
struct AtUnit { bf16x8 q0, q1; f32x16 negm, o; f32x4 l; };
DI void at_exp_pack(f32x16 s, u32x4& p0, u32x4& p1) {
#pragma unroll
    for (int r = 0; r < 16; ++r) s[r] = __builtin_amdgcn_exp2f(s[r]);
    p0 = (u32x4){pk2(s[0], s[1]), pk2(s[2], s[3]), pk2(s[4], s[5]), pk2(s[6], s[7])};
    p1 = (u32x4){pk2(s[8], s[9]), pk2(s[10], s[11]), pk2(s[12], s[13]), pk2(s[14], s[15])};
}
DI f32x16 at_scores(const AtUnit& u, bf16x8 k0, bf16x8 k1) { return MFMA32(k1, u.q1, MFMA32(k0, u.q0, u.negm)); }
DI void at_accum(AtUnit& u, f32x16 s, bf16x8 v0, bf16x8 v1, bf16x8 ones) {
    u32x4 p0, p1; at_exp_pack(s, p0, p1);
    u.o = MFMA32(v0, BF8(p0), u.o); u.l = MFMA16(ones, BF8(p0), u.l);
    u.o = MFMA32(v1, BF8(p1), u.o); u.l = MFMA16(ones, BF8(p1), u.l);
}
DI bf16x8 at_v(const AtAddr& a, unsigned off) { return cat8(ldstr(a.v0 + off), ldstr(a.v1 + off)); }
DI void at_single(AtUnit& u, const AtAddr& a, int kt, bf16x8 ones) {
    const unsigned o = kt * 2048;
    const f32x16 s = at_scores(u, lds128(a.k0 + o), lds128(a.k1 + o));
    at_accum(u, s, at_v(a, o), at_v(a, o + 1024), ones);
}
#define AT_EXP4(s_, i) do { s_[i] = __builtin_amdgcn_exp2f(s_[i]); s_[i + 1] = __builtin_amdgcn_exp2f(s_[i + 1]); s_[i + 2] = __builtin_amdgcn_exp2f(s_[i + 2]); s_[i + 3] = __builtin_amdgcn_exp2f(s_[i + 3]); } while (0)
#define AT_G(MF, s_, i, p_) do { MF; AT_EXP4(s_, i); p_[((i) & 7) / 2] = pk2(s_[i], s_[i + 1]); p_[((i) & 7) / 2 + 1] = pk2(s_[i + 2], s_[i + 3]); PINV(p_); SBAR(); } while (0)
DI void at_body(AtUnit& x, AtUnit& y, const AtAddr& a, bf16x8 (&kf)[4], int kt, int ktn, bf16x8 ones) {
    unsigned va = a.v0 + kt * 2048, vb = a.v1 + kt * 2048, ka = a.k0 + ktn * 2048, kb = a.k1 + ktn * 2048;
    asm volatile("" : "+v"(va), "+v"(vb), "+v"(ka), "+v"(kb));
    SBAR();
    bf16x8 vf[4];
#pragma unroll
    for (int i = 0; i < 4; ++i) vf[i] = cat8(ldstr(va + i * 1024), ldstr(vb + i * 1024));
    f32x16 s0 = MFMA32(kf[0], x.q0, x.negm); s0 = MFMA32(kf[1], x.q1, s0);
    f32x16 s1 = MFMA32(kf[0], y.q0, y.negm), s2, s3; PINV(s0); PINV(s1); SBAR();
    u32x4 p0, p1, p2, p3;
    AT_G(s1 = MFMA32(kf[1], y.q1, s1); PINV(s1), s0, 0, p0);
    AT_G(s2 = MFMA32(kf[2], x.q0, x.negm); PINV(s2), s0, 4, p0);
    AT_G(s2 = MFMA32(kf[3], x.q1, s2); PINV(s2), s0, 8, p1);
    AT_G(s3 = MFMA32(kf[2], y.q0, y.negm); PINV(s3), s0, 12, p1);
    AT_G(s3 = MFMA32(kf[3], y.q1, s3); PINV(s3), s1, 0, p2);
    kf[0] = lds128(ka); kf[1] = lds128(kb); kf[2] = lds128(ka + 2048); kf[3] = lds128(kb + 2048);
    AT_G(x.o = MFMA32(vf[0], BF8(p0), x.o); PINV(x.o), s1, 4, p2);
    AT_G(x.o = MFMA32(vf[1], BF8(p1), x.o); PINV(x.o), s1, 8, p3);
    AT_G(x.l = MFMA16(ones, BF8(p0), x.l); x.l = MFMA16(ones, BF8(p1), x.l); PINV(x.l), s1, 12, p3);
    AT_G(y.o = MFMA32(vf[0], BF8(p2), y.o); PINV(y.o), s2, 0, p0);
    AT_G(y.o = MFMA32(vf[1], BF8(p3), y.o); PINV(y.o), s2, 4, p0);
    AT_G(y.l = MFMA16(ones, BF8(p2), y.l); y.l = MFMA16(ones, BF8(p3), y.l); PINV(y.l), s2, 8, p1);
    AT_G(x.o = MFMA32(vf[2], BF8(p0), x.o); PINV(x.o), s2, 12, p1);
    AT_G(x.l = MFMA16(ones, BF8(p0), x.l); PINV(x.l), s3, 0, p2);
    AT_G(x.o = MFMA32(vf[3], BF8(p1), x.o); PINV(x.o), s3, 4, p2);
    AT_G(x.l = MFMA16(ones, BF8(p1), x.l); y.o = MFMA32(vf[2], BF8(p2), y.o); PINV(x.l); PINV(y.o), s3, 8, p3);
    AT_G(y.l = MFMA16(ones, BF8(p2), y.l); PINV(y.l), s3, 12, p3);
    y.o = MFMA32(vf[3], BF8(p3), y.o); y.l = MFMA16(ones, BF8(p3), y.l);
    SBAR();
}
DI void at_kload(bf16x8 (&kf)[4], const AtAddr& a, int kt) { const unsigned ka = a.k0 + kt * 2048, kb = a.k1 + kt * 2048; kf[0] = lds128(ka); kf[1] = lds128(kb); kf[2] = lds128(ka + 2048); kf[3] = lds128(kb + 2048); }
DI void at_masks(u32x4& md0, u32x4& md1, int ql, int hi) {
#pragma unroll
    for (int r = 0; r < 4; ++r) {
        md0[r] = (crow(2 * r, hi) <= ql ? 0xffffu : 0u) | (crow(2 * r + 1, hi) <= ql ? 0xffff0000u : 0u);
        md1[r] = (crow(8 + 2 * r, hi) <= ql ? 0xffffu : 0u) | (crow(9 + 2 * r, hi) <= ql ? 0xffff0000u : 0u);
    }
}
template <bool FAR> DI void at_edges(AtUnit& u, const AtAddr& a, int t, bf16x8 ones, int ql, int hi) {
    const unsigned od = t * 2048, of = od + 16384;
    const bf16x8 kd0 = lds128(a.k0 + od), kd1 = lds128(a.k1 + od), vd0 = at_v(a, od), vd1 = at_v(a, od + 1024);
    bf16x8 kf0, kf1, vf0, vf1;
    if (FAR) { kf0 = lds128(a.k0 + of); kf1 = lds128(a.k1 + of); vf0 = at_v(a, of); vf1 = at_v(a, of + 1024); }
    SBAR();
    f32x16 s = at_scores(u, kd0, kd1);
    if (FAR) {
        const f32x16 sf = at_scores(u, kf0, kf1);
#pragma unroll
        for (int r = 0; r < 16; ++r) s[r] = (crow(r, hi) <= ql) ? s[r] : sf[r];
    } else at_mask<1>(s, ql, hi);
    u32x4 p0, p1; at_exp_pack(s, p0, p1);
    u.l = MFMA16(ones, BF8(p0), u.l); u.l = MFMA16(ones, BF8(p1), u.l);
    if (FAR) {
        u32x4 md0, md1; at_masks(md0, md1, ql, hi);
        const u32x4 d0 = p0 & md0, d1 = p1 & md1;
        u.o = MFMA32(vd0, BF8(d0), u.o); u.o = MFMA32(vd1, BF8(d1), u.o);
        u.o = MFMA32(vf0, BF8(p0 ^ d0), u.o); u.o = MFMA32(vf1, BF8(p1 ^ d1), u.o);
    } else { u.o = MFMA32(vd0, BF8(p0), u.o); u.o = MFMA32(vd1, BF8(p1), u.o); }
}
DI void at_edge_block(AtUnit& x, AtUnit& y, const AtAddr& a, const bf16x8 (&kp)[4], int t, bf16x8 ones, int ql, int hi) {
    bf16x8 kd[4], vd[4], vp[4];
    at_kload(kd, a, t);
    unsigned va = a.v0 + t * 2048, vb = a.v1 + t * 2048; PINV(va); PINV(vb);
    vp[2] = cat8(ldstr(va + 16384 + 2048), ldstr(vb + 16384 + 2048)); vp[3] = cat8(ldstr(va + 16384 + 3072), ldstr(vb + 16384 + 3072));
    SBAR();
    f32x16 sw = at_scores(x, kp[2], kp[3]);
    const f32x16 sxf = at_scores(x, kp[0], kp[1]); f32x16 sx = at_scores(x, kd[0], kd[1]);
    at_accum(x, sw, vp[2], vp[3], ones);
    SBAR();
    const f32x16 syf = at_scores(y, kp[2], kp[3]); f32x16 sy = at_scores(y, kd[2], kd[3]);
#pragma unroll
    for (int i = 0; i < 4; ++i) vd[i] = cat8(ldstr(va + i * 1024), ldstr(vb + i * 1024));
    vp[0] = cat8(ldstr(va + 16384), ldstr(vb + 16384)); vp[1] = cat8(ldstr(va + 16384 + 1024), ldstr(vb + 16384 + 1024));
#pragma unroll
    for (int r = 0; r < 16; ++r) sx[r] = (crow(r, hi) <= ql) ? sx[r] : sxf[r];
    u32x4 p0, p1, md0, md1; at_exp_pack(sx, p0, p1); at_masks(md0, md1, ql, hi);
    x.l = MFMA16(ones, BF8(p0), x.l); x.l = MFMA16(ones, BF8(p1), x.l);
    { const u32x4 d0 = p0 & md0, d1 = p1 & md1;
      x.o = MFMA32(vd[0], BF8(d0), x.o); x.o = MFMA32(vd[1], BF8(d1), x.o); x.o = MFMA32(vp[0], BF8(p0 ^ d0), x.o); x.o = MFMA32(vp[1], BF8(p1 ^ d1), x.o); }
#pragma unroll
    for (int r = 0; r < 16; ++r) sy[r] = (crow(r, hi) <= ql) ? sy[r] : syf[r];
    at_exp_pack(sy, p0, p1);
    y.l = MFMA16(ones, BF8(p0), y.l); y.l = MFMA16(ones, BF8(p1), y.l);
    { const u32x4 d0 = p0 & md0, d1 = p1 & md1;
      y.o = MFMA32(vd[2], BF8(d0), y.o); y.o = MFMA32(vd[3], BF8(d1), y.o); y.o = MFMA32(vp[2], BF8(p0 ^ d0), y.o); y.o = MFMA32(vp[3], BF8(p1 ^ d1), y.o); }
}
DI AtAddr at_addr(unsigned lds0, int hd, int lane) {
    const int ql = lane & 31, hi = lane >> 5, blk = (lane >> 4) & 1, q = (lane & 15) >> 2, p = lane & 3, c = 2 * blk + (p >> 1);
    AtAddr a; a.k0 = lds0 + AT_K + hd * 32768 + ql * 64 + ((hi ^ ((ql >> 2) & 3)) << 4); a.k1 = a.k0 ^ 32u;
    const unsigned vb = lds0 + AT_V + hd * 32768 + (4 * hi + q) * 64 + 8 * (p & 1);
    a.v0 = vb + ((c ^ hi) << 4); a.v1 = vb + 512 + ((c ^ (2 + hi)) << 4);
    asm volatile("" : "+v"(a.k0), "+v"(a.k1), "+v"(a.v0), "+v"(a.v1));
    return a;
}
DI void at_init(AtUnit& x, AtUnit& y, const AtAddr& a, unsigned qa, int t, bf16x8 ones) {
    unsigned q0 = qa + t * 2048, q1 = q0 ^ 32u, k0 = a.k0 + t * 2048, k1 = a.k1 + t * 2048, v0 = a.v0 + t * 2048, v1 = a.v1 + t * 2048;
    asm volatile("" : "+v"(q0), "+v"(q1), "+v"(k0), "+v"(k1), "+v"(v0), "+v"(v1));
    const bf16x8 kf0 = lds128(k0), kf1 = lds128(k1);
    y.q0 = lds128(q0 + 2048); y.q1 = lds128(q1 + 2048); x.q0 = lds128(q0); x.q1 = lds128(q1);
    const bf16x8 vf0 = cat8(ldstr(v0), ldstr(v1)), vf1 = cat8(ldstr(v0 + 1024), ldstr(v1 + 1024));
    SBAR();
    const f32x16 z = {};
    f32x16 sy = MFMA32(kf1, y.q1, MFMA32(kf0, y.q0, z)); const f32x16 sx = MFMA32(kf1, x.q1, MFMA32(kf0, x.q0, z));
    const float my = __uint_as_float(__builtin_amdgcn_permlane32_swap(__float_as_uint(sy[0]), __float_as_uint(sy[0]), false, false)[0]);
    const float mx = __uint_as_float(__builtin_amdgcn_permlane32_swap(__float_as_uint(sx[0]), __float_as_uint(sx[0]), false, false)[0]);
    y.negm = splat16(-my); x.negm = splat16(-mx);
    x.o = z; x.l = (f32x4){0.f, 0.f, 0.f, 0.f}; y.o = z; y.l = x.l;
    at_accum(y, sy + y.negm, vf0, vf1, ones);
}
DI void at_finish(AtUnit& u, unsigned lds0, int hd, int t, bool has_prev, bf16x8 ones, rsrc_t srdO, unsigned voff) {
    if (__builtin_expect(__any(!(u.l[0] < AT_LMAX)), 0)) {
        const int lane = lane_again(), ql = lane & 31, hi = lane >> 5; const AtAddr a = at_addr(lds0, hd, lane);
        float mx = at_max_tile<1>(a, t, u.q0, u.q1, -INFINITY, ql, hi);
        for (int kt = 0; kt < t; ++kt) mx = at_max_tile<0>(a, kt, u.q0, u.q1, mx, ql, hi);
        if (has_prev) {
            mx = at_max_tile<2>(a, 8 + t, u.q0, u.q1, mx, ql, hi);
            for (int kt = 9 + t; kt < 16; ++kt) mx = at_max_tile<0>(a, kt, u.q0, u.q1, mx, ql, hi);
        }
        u.negm = splat16(-xhalf_max(mx)); u.o = splat16(0.f); u.l = (f32x4){0.f, 0.f, 0.f, 0.f};
        for (int kt = 0; kt < t; ++kt) at_single(u, a, kt, ones);
        if (has_prev) { at_edges<true>(u, a, t, ones, ql, hi); for (int kt = 9 + t; kt < 16; ++kt) at_single(u, a, kt, ones); }
        else at_edges<false>(u, a, t, ones, ql, hi);
    }
    const float rl = __builtin_amdgcn_rcpf(u.l[0]);
    u32x4 c[2];
#pragma unroll
    for (int k = 0; k < 2; ++k) {
        unsigned a0 = pk2(u.o[8 * k] * rl, u.o[8 * k + 1] * rl), a1 = pk2(u.o[8 * k + 2] * rl, u.o[8 * k + 3] * rl);
        unsigned b0 = pk2(u.o[8 * k + 4] * rl, u.o[8 * k + 5] * rl), b1 = pk2(u.o[8 * k + 6] * rl, u.o[8 * k + 7] * rl);
        auto r0 = __builtin_amdgcn_permlane32_swap(a0, b0, false, false);
        auto r1 = __builtin_amdgcn_permlane32_swap(a1, b1, false, false);
        c[k] = (u32x4){r0[0], r1[0], r0[1], r1[1]};
    }
#pragma unroll
    for (int i = 0; i < 4; ++i) { auto r = __builtin_amdgcn_permlane16_swap(c[0][i], c[1][i], false, false); c[0][i] = r[0]; c[1][i] = r[1]; }
    st16c(c[0], srdO, voff, (unsigned)(32 * t) * (KT_ * 2));
    st16c(c[1], srdO, voff, (unsigned)(32 * t + 16) * (KT_ * 2));
}

DI void wait_tags(const u64* p, bool need, u64 tag) {
    for (unsigned spins = 0; spins < (1u << 20); ++spins) {
        const u64 v = __hip_atomic_load((GAS const u64*)p, RLX_AGENT);
        if (__all(!need || v == tag)) break;
        __builtin_amdgcn_s_sleep(1);
    }
    __builtin_amdgcn_fence(__ATOMIC_ACQUIRE, "wavefront");
}

__global__ __launch_bounds__(512, 2) void fused_block(Ptrs P0) {
    extern __shared__ __attribute__((aligned(16))) char smem[];
    const unsigned lds0 = (unsigned)(size_t)(LAS char*)smem;
    const int w = __builtin_amdgcn_readfirstlane(threadIdx.x >> 6);
    u64* const st = (u64*)(P0.ws + OFF_ST);
    STAMP(0, 0); STAMPC(0, 8);
    u64 tag;
    {
        const Ptrs P = P0;
        const int xcd = blockIdx.x & 7, idx = blockIdx.x >> 3;
        u64* const flags = (u64*)(P.ws + OFF_FL);
        const int pm = 4 * (xcd & 1) + (idx & 3), j = 4 * (xcd >> 1) + ((idx >> 2) & 3);
        const int wr = w >> 1, wc = w & 1;
        if (idx >= 16) {
            f32x4 acc[4][7];
            k1_mainloop<7>(acc, lds0, P, (unsigned)(pm * 256 * 2048), (unsigned)(j * 448), w, lane_again(), pm, flags, tag, st);
            STAMP(0, 1); STAMPC(0, 9);
            const int lane = lane_again(), fr = lane & 15, fq = lane >> 4;
            int cy[4];
#pragma unroll
            for (int y = 0; y < 4; ++y) cy[y] = 16 * k1_pair<7>(y, wr, wc) + (y < 3 ? 8 : 4) * fq;
            const rsrc_t srdHO = mkrsrc(args_again().ws + OFF_HO, S_ * KT_ * 2);
            f32x4 bf[7], cs[7]; f32x2 rr[4];
#pragma unroll
            for (int n = 0; n < 7; ++n) {
                const u32x2 bb = *(const LAS u32x2*)(size_t)(lds0 + TAB_B + (cy[n >> 1] + 4 * (n & 1)) * 2);
                bf[n] = (f32x4){blo(bb[0]), bhi(bb[0]), blo(bb[1]), bhi(bb[1])}; cs[n] = *(const LAS f32x4*)(size_t)(lds0 + TAB_COL + (cy[n >> 1] + 4 * (n & 1)) * 4);
            }
#pragma unroll
            for (int m = 0; m < 4; ++m) rr[m] = *(const LAS f32x2*)(size_t)(lds0 + TAB_ROW + (64 * wr + 16 * m + fr) * 8);
#define HVAL(m, n) gelu4(fma4(acc[m][n], sp4(rr[m][0]), fma4(cs[n], sp4(-rr[m][1]), bf[n])))
            const unsigned vo = (unsigned)((pm * 256 + 64 * wr + fr) * KT_ + 224 * j) * 2u;
#pragma unroll
            for (int m = 0; m < 4; ++m) { const f32x4 hv = HVAL(m, 6); *(LAS u32x2*)(size_t)(lds0 + (64 * wr + 16 * m + fr) * 80 + 32 * wc + 8 * fq) = (u32x2){pk2(hv[0], hv[1]), pk2(hv[2], hv[3])}; }
            SBAR();
#pragma unroll
            for (int m = 0; m < 4; ++m) {
#pragma unroll
                for (int y = 0; y < 3; ++y) { st16c(pack8(HVAL(m, 2 * y), HVAL(m, 2 * y + 1)), srdHO, vo + cy[y] * 2, 16 * m * KT_ * 2); SBAR(); }
                if (m == 1) {
                    WAITL0(); BAR();
#pragma unroll
                    for (int i = 0; i < 2; ++i) st16c(*(const LAS u32x4*)(size_t)(lds0 + (64 * wr + 32 * wc + 16 * i + fr) * 80 + 16 * fq), srdHO, vo + 192 + 16 * fq, (32 * wc + 16 * i) * KT_ * 2);
                    SBAR();
                }
            }
            STAMP(0, 2);
        } else {
            f32x4 acc[4][6];
            k1_mainloop<6>(acc, lds0, P, (unsigned)(pm * 256 * 2048), (unsigned)(j * 128), w, lane_again(), pm, flags, tag, st);
            STAMP(0, 1); STAMPC(0, 9);
            const int lane = lane_again(), fr = lane & 15, fq = lane >> 4;
            {
                f32x2 rr[4];
#pragma unroll
                for (int m = 0; m < 4; ++m) rr[m] = *(const LAS f32x2*)(size_t)(lds0 + TAB_ROW + (64 * wr + 16 * m + fr) * 8);
                const rsrc_t srdX = mkrsrc(P.ws + OFF_KV + (size_t)(pm * 16 + j) * 65536, 65536);
                const unsigned rowoff = (unsigned)((64 * wr + fr) * 64 + ((fq ^ ((fr >> 2) & 3)) << 4));
                f32x4 cc[6];
#pragma unroll
                for (int n = 0; n < 6; ++n) cc[n] = *(const LAS f32x4*)(size_t)(lds0 + TAB_COL + (16 * k1_pair<6>(n >> 1, wr, wc) + 8 * fq + 4 * (n & 1)) * 4);
                SBAR();
#pragma unroll
                for (int y = 0; y < 3; ++y) {
                    const int blk = k1_pair<6>(y, wr, wc) >> 1;
                    const unsigned lb = lds0 + (blk < 2 ? AT_Q + blk * 16384 : AT_K + (blk - 2) * 32768) + rowoff;
                    const float sc = blk < 2 ? 0.17677669529663687f * 1.4426950408889634f : 1.0f;
                    u32x4 v[4];
#pragma unroll
                    for (int m = 0; m < 4; ++m) {
                        const f32x4 r_ = sp4(rr[m][0] * sc), t_ = sp4(-rr[m][1] * sc);
                        v[m] = pack8(fma4(acc[m][2 * y], r_, cc[2 * y] * t_), fma4(acc[m][2 * y + 1], r_, cc[2 * y + 1] * t_));
                        *(LAS u32x4*)(size_t)(lb + m * 1024) = v[m];
                    }
                    if (blk >= 2 && pm < 7) {
#pragma unroll
                        for (int m = 0; m < 4; ++m) st16c(v[m], srdX, rowoff + (unsigned)(blk - 2) * 16384u, m * 1024);
                    }
                }
            }
            const int hd = w >> 2, uu = hd ? 7 - w : w, ta = 2 * uu, tb = ta + 1;
            const AtAddr ad = at_addr(lds0, hd, lane);
            const short one = (((lane >> 4) & 1) == ((lane >> 2) & 1)) ? (short)0x3f80 : (short)0;
            bf16x8 ones = {one, one, one, one, one, one, one, one}; PINV(ones);
            WAITL0(); BAR();
            STAMP(0, 6);
            AtUnit ua, ub;
            bf16x8 kf[4];
            at_kload(kf, ad, 0);
            at_init(ua, ub, ad, ad.k0 - AT_K + AT_Q - hd * 16384, ta, ones);
            WAITV(0);
            if (lane == 0) __hip_atomic_store((GAS u64*)(flags + FL_KV + (pm * 16 + j) * 16 + w), tag, RLX_AGENT);
            STAMP(0, 2);
            for (int i = 0; i < uu; ++i) at_body(ua, ub, ad, kf, 2 * i, 2 * i + 2, ones);
            if (pm > 0) {
                if (uu < 2) {
                    const int lane = lane_again();
                    wait_tags(flags + FL_KV + ((pm - 1) * 16 + j) * 16 + (lane & 7), true, tag);
                    const rsrc_t srdX = mkrsrc(P.ws + OFF_KV + (size_t)((pm - 1) * 16 + j) * 65536, 65536);
                    const int rg = w & 3;
#pragma unroll
                    for (int q = 0; q < 16; ++q) dma16c(lds0 + AT_K + rg * 32768 + 16384 + q * 1024, lane * 16, srdX, rg * 16384 + q * 1024);
                }
                STAMP(0, 3);
                WAITVL_BAR(0);
                STAMP(0, 4);
                at_kload(kf, ad, 14);
                for (int i = 3; i > uu; --i) at_body(ua, ub, ad, kf, 8 + 2 * i, 6 + 2 * i, ones);
                const int l2 = lane_again();
                at_edge_block(ua, ub, ad, kf, ta, ones, l2 & 31, l2 >> 5);
            } else { const int l2 = lane_again(); const AtAddr a2 = at_addr(lds0, hd, l2); at_edges<false>(ua, a2, ta, ones, l2 & 31, l2 >> 5); at_edges<false>(ub, a2, tb, ones, l2 & 31, l2 >> 5); }
            const rsrc_t srdHO = mkrsrc(args_again().ws + OFF_HO, S_ * KT_ * 2);
            const int l3 = lane_again();
            const unsigned vo = (unsigned)((pm * 256 + (l3 & 15)) * KT_ + F_ + 64 * j + 32 * hd + (l3 & 16) + 8 * (l3 >> 5)) * 2u;
            at_finish(ua, lds0, hd, ta, pm > 0, ones, srdHO, vo);
            at_finish(ub, lds0, hd, tb, pm > 0, ones, srdHO, vo);
            STAMP(0, 5);
        }
    }
    const Ptrs P = args_again();
    const int lane = lane_again(), fr = lane & 15, fq = lane >> 4;
    const int bid = bid_again(), xcd = bid & 7, idx = bid >> 3;
    u64* const flags = (u64*)(P.ws + OFF_FL);
    const int pm = 4 * (xcd & 1) + (idx & 3), j = 4 * (xcd >> 1) + ((idx >> 2) & 3);
    const int h = xcd & 1, rt = p2_tile(bid) >> 3, ct = p2_tile(bid) & 7;
    const rsrc_t srdHO = mkrsrc(P.ws + OFF_HO, S_ * KT_ * 2);
    const int g = w >> 2, w4 = w & 3, wr = w4 >> 1, wc = w4 & 1;
    const rsrc_t srdW2 = mkrsrc(P.w2, F_ * D_ * 2), srdWo = mkrsrc(P.wo, F_ * D_ * 2);
    const unsigned voffA = (unsigned)((8 * w4 + (lane >> 3)) * (KT_ * 2) + (((lane & 7) ^ (4 * (w4 & 1) + (lane >> 4))) << 4));
    const unsigned voffB = (unsigned)((4 * w4 + (lane >> 4)) * 2048 + (((lane & 15) ^ (((lane >> 4) | ((w4 >> 1) << 2)) << 1)) << 4));
    const unsigned soffA0 = (unsigned)((rt * 128 + 32 * h) * (KT_ * 2) + h * 36 * 128), soffB0 = (unsigned)(ct * 256);
    const unsigned dA1 = (unsigned)((h ? -32 : 32) * (KT_ * 2));
    const unsigned ldsW = lds0 + 1024u * w4;
    WAITL0(); BAR();
    STAMP(1, 0); STAMPC(1, 8);
#define K2_B0(tau) do { _Pragma("unroll") for (int i = 0; i < 2; ++i) \
        dma16(ldsW + (tau) * 32768 + 16384 + (2 * g + i) * 4096, voffB, srdW2, soffB0 + (unsigned)((36 * h + (tau)) * 64 + 16 * (2 * g + i)) * 2048u); } while (0)
    K2_B0(0); K2_B0(1);
    WAITV_BAR(4);
    if (w == 0) {
        const int slot = (idx >= 16) ? j : 16 + j;
        if (lane == 0) __hip_atomic_store((GAS u64*)(flags + FL_P1 + pm * 32 + slot), tag, RLX_AGENT);
        STAMP(1, 5);
    }
    K2_B0(2); K2_B0(3); K2_B0(4);
    if (w == 0) {
        const int slot = (idx >= 16) ? j : 16 + j;
        const bool need = lane >= 32 || (h ? lane >= 10 : lane <= 10);
        wait_tags(flags + FL_P1 + (lane < 32 ? (rt >> 1) * 32 + lane : (pm ^ 4) * 32 + slot), need, tag);
    }
    const unsigned ra0 = lds0 + (64 * wr + fr) * 128 + ((fq ^ (fr >> 1)) << 4), ra1 = ra0 ^ 64u;
    unsigned rb[4];
    {
        const int q = (lane & 15) >> 2, p = lane & 3, s = q | ((fq & 1) << 2);
#pragma unroll
        for (int n = 0; n < 4; ++n) rb[n] = lds0 + 16384 + (8 * fq + q) * 256 + (((4 * wc + n) ^ s) << 5) + 8 * p;
    }
    f32x4 acc[4][4];
    bf16x8 xf[2][2][4]; s16x4 wl[2][2][4], wh[2][2][4];
    unsigned ca0, ca1, cb[4];
#define K2_ADDR(buf) do { const unsigned o_ = (buf) * 32768; ca0 = ra0 + o_; ca1 = ra1 + o_; _Pragma("unroll") for (int i = 0; i < 4; ++i) cb[i] = rb[i] + o_; \
        asm volatile("" : "+v"(ca0), "+v"(ca1), "+v"(cb[0]), "+v"(cb[1]), "+v"(cb[2]), "+v"(cb[3])); } while (0)
#define K2_RD1(S, r) do { constexpr int f_ = (r) / 3, k_ = f_ >> 2, n_ = f_ & 3; \
        if ((r) % 3 == 0) wl[S][k_][n_] = ldstr(cb[n_] + k_ * 8192); else if ((r) % 3 == 1) wh[S][k_][n_] = ldstr(cb[n_] + k_ * 8192 + 1024); \
        else xf[S][k_][n_] = lds128((k_ ? ca1 : ca0) + n_ * 2048); } while (0)
#define K2_MM(S) do { SBAR(); \
        unroll_for<32>([&](auto J) { constexpr int j = J.value, ks = j >> 4, n = (j >> 2) & 3, m = (n & 1) ? 3 - (j & 3) : (j & 3); \
            acc[m][n] = MFMA16(cat8(wl[S][ks][n], wh[S][ks][n]), xf[S][ks][m], acc[m][n]); \
            if constexpr (j < 24) K2_RD1((S) ^ 1, j); SBAR(); }); } while (0)
#define K2_STEP(S) do { \
        const unsigned l_ = lw, sa_ = sa, sb_ = sb; const rsrc_t rb_ = srdB; \
        dma16(l_ + 16384, voffB, rb_, sb_); \
        dma16c(l_, voffA, srdHO, sa_); \
        bc = (bc >= 3) ? bc - 3 : bc + 2; PINS(bc); K2_ADDR(bc); \
        dma16(l_ + 16384 + 4096, voffB, rb_, sb_ + 16 * 2048); \
        kt += 2; lw = ldsW + bc * 32768; sa = (kt < ktend) ? sa + 256 : OOB; PINS(kt); PINS(lw); PINS(sa); \
        dma16c(l_ + 4096, voffA, srdHO, sa_ + dA1); \
        sb = (kt < ktend) ? soffB0 + (unsigned)((kt >= 56 ? kt - 56 : kt) * (64 * 2048)) : OOB; srdB = (kt >= 56) ? srdWo : srdW2; PINS(sb); PINS(srdB); \
        dma16(l_ + 16384 + 8192, voffB, rb_, sb_ + 32 * 2048); \
        dma16c(l_ + 8192, voffA, srdHO, sa_ + 64 * KT_ * 2); \
        dma16(l_ + 16384 + 12288, voffB, rb_, sb_ + 48 * 2048); \
        dma16c(l_ + 12288, voffA, srdHO, sa_ + 64 * KT_ * 2 + dA1); \
        BAR(); K2_MM(S); WAITVL_BAR(8); SBAR(); } while (0)
    K2_ADDR(g);
#pragma unroll
    for (int m = 0; m < 4; ++m)
#pragma unroll
        for (int n = 0; n < 4; ++n) { acc[m][n] = (f32x4){0.f, 0.f, 0.f, 0.f}; PINV(acc[m][n]); }
    BAR();
    STAMP(1, 6);
#define K2_A0(tau) do { const unsigned s_ = soffA0 + (unsigned)(g * (64 * KT_ * 2) + (tau) * 128), l_ = ldsW + (tau) * 32768 + g * 8192; \
        dma16c(l_, voffA, srdHO, s_); dma16c(l_ + 4096, voffA, srdHO, s_ + dA1); } while (0)
    K2_A0(0); K2_A0(1); K2_A0(2); K2_A0(3);
    WAITV_BAR(4);
    unroll_for<24>([&](auto J) { K2_RD1(0, J.value); });
    K2_A0(4);
    WAITVL_BAR(4);
    if (g) WAITV_BAR(0);
    STAMPC(1, 10);
    int bc = g, kt = 36 * h + g + 5;
    const int ktend = 36 * h + 36;
    unsigned lw = ldsW + g * 32768, sa = soffA0 + (g + 5) * 128, sb = soffB0 + (unsigned)(kt * (64 * 2048));
    rsrc_t srdB = srdW2;
    for (int it = 0; it < 8; ++it) { K2_STEP(0); K2_STEP(1); }
    bc = (bc >= 3) ? bc - 3 : bc + 2; K2_ADDR(bc); BAR(); K2_MM(0); WAITVL_BAR(0); SBAR();
    const int lane3 = lane_again();
    const unsigned xa = lds0 + 32768 + w4 * 8192 + lane3 * 16;
    if (!g) {
        const int fr3 = lane3 & 15, fq3 = lane3 >> 4;
        const int row = rt * 128 + 64 * wr + 32 * (1 - h) + fr3, col = ct * 128 + 64 * wc;
        u32x2 xv[2][4], bv[4];
#pragma unroll
        for (int n = 0; n < 4; ++n) {
            bv[n] = *(const u32x2*)(P.b2 + col + 16 * n + 4 * fq3);
#pragma unroll
            for (int m = 0; m < 2; ++m) xv[m][n] = *(const u32x2*)(P.x + (size_t)(row + 16 * m) * D_ + col + 16 * n + 4 * fq3);
        }
        BAR(); K2_MM(1); BAR(); SBAR();
#pragma unroll
        for (int i = 0; i < 8; ++i) *(LAS f32x4*)(size_t)(xa + i * 1024) = acc[i >> 2][i & 3];
        if (bid == 0 && w == 1) {
            for (unsigned spins = 0; spins < (1u << 20); ++spins) {
                bool ok = true;
#pragma unroll
                for (int i = 0; i < 4; ++i) ok = ok & (__hip_atomic_load((GAS const u64*)(flags + FL_P1 + i * 64 + lane3), RLX_AGENT) == tag);
                if (__all(ok)) break;
            }
            if (lane3 == 0) __hip_atomic_store((GAS u64*)flags, tag, RLX_AGENT);
        }
        WAITL0(); BAR();
        STAMP(1, 1); STAMPC(1, 9);
#pragma unroll
        for (int i = 0; i < 8; ++i) { const int m = i >> 2, n = i & 3;
            acc[2 + m][n] += (f32x4){blo(xv[m][n][0]) + blo(bv[n][0]), bhi(xv[m][n][0]) + bhi(bv[n][0]), blo(xv[m][n][1]) + blo(bv[n][1]), bhi(xv[m][n][1]) + bhi(bv[n][1])}; }
        const rsrc_t srdR = px_rsrc(P.ws, bid, 1);
        BAR();
#pragma unroll
        for (int i = 0; i < 8; ++i) acc[2 + (i >> 2)][i & 3] += *(const LAS f32x4*)(size_t)(xa + 32768 + i * 1024);
        for (unsigned spins = 0; spins < (1u << 20); ++spins) {
            if (__builtin_amdgcn_readfirstlane(__builtin_amdgcn_raw_buffer_load_b32(srdR, w4 * 8192 + 8188, 0, 16)) != ~0u) break;
            __builtin_amdgcn_s_sleep(1);
        }
        STAMP(1, 7);
        u32x4 r[8];
        for (unsigned spins = 0; spins < (1u << 20); ++spins) {
            unsigned all = 0;
#pragma unroll
            for (int i = 0; i < 8; ++i) r[i] = __builtin_amdgcn_raw_buffer_load_b128(srdR, lane3 * 16 + w4 * 8192 + i * 1024, 0, 16);
#pragma unroll
            for (int i = 0; i < 8; ++i) all = max(max(all, max(r[i][0], r[i][1])), max(r[i][2], r[i][3]));
            if (__all(all != ~0u)) break;
            asm volatile("" ::: "memory");
        }
        STAMP(1, 3);
#pragma unroll
        for (int i = 0; i < 8; ++i) acc[2 + (i >> 2)][i & 3] += __builtin_bit_cast(f32x4, r[i]);
        u16* op = P.out + (size_t)row * D_ + col + 16 * (fq3 & 1) + 8 * (fq3 >> 1);
#pragma unroll
        for (int m = 0; m < 2; ++m) { *(u32x4*)(op + m * 16 * D_) = pair16(acc[2 + m][0], acc[2 + m][1]); *(u32x4*)(op + m * 16 * D_ + 32) = pair16(acc[2 + m][2], acc[2 + m][3]); }
    } else {
        BAR(); K2_MM(1); BAR(); SBAR();
        STAMP(1, 1); STAMPC(1, 9);
        const rsrc_t srdS = px_rsrc(P.ws, bid, 0);
        f32x4 pv[8];
#pragma unroll
        for (int i = 0; i < 8; ++i) pv[i] = *(const LAS f32x4*)(size_t)(xa + i * 1024);
        SBAR();
#pragma unroll
        for (int i = 0; i < 8; ++i) {
            const f32x4 sv = acc[i >> 2][i & 3] + pv[i];
            st16c(__builtin_elementwise_min(__builtin_bit_cast(u32x4, sv), (u32x4){~1u, ~1u, ~1u, ~1u}), srdS, lane3 * 16 + w4 * 8192, i * 1024);
        }
        STAMP(1, 2);
#pragma unroll
        for (int i = 0; i < 8; ++i) *(LAS f32x4*)(size_t)(xa + 32768 + i * 1024) = acc[2 + (i >> 2)][i & 3];
        WAITL0(); BAR();
    }
    STAMP(1, 4);
}

extern "C" void kernel_launch(void* const* d_in, const int* in_sizes, int n_in, void* d_out, int out_size, void* d_ws, size_t ws_size, hipStream_t stream) {
    Ptrs P;
    P.x = (const u16*)d_in[0]; P.wqkv = (const u16*)d_in[1]; P.wo = (const u16*)d_in[2];
    P.w1 = (const u16*)d_in[4]; P.b1 = (const u16*)d_in[5]; P.w2 = (const u16*)d_in[6]; P.b2 = (const u16*)d_in[7];
    P.out = (u16*)d_out; P.ws = (char*)d_ws;
    (void)hipFuncSetAttribute((const void*)fused_block, hipFuncAttributeMaxDynamicSharedMemorySize, LDS_BYTES);
    fused_block<<<256, 512, LDS_BYTES, stream>>>(P);
}
```

```cpp
#include <hip/hip_runtime.h>
#include <hip/hip_bf16.h>

typedef unsigned short u16;
typedef unsigned long long u64;
typedef short bf16x8 __attribute__((ext_vector_type(8)));
typedef short s16x4 __attribute__((ext_vector_type(4)));
typedef float f32x4 __attribute__((ext_vector_type(4)));
typedef float f32x16 __attribute__((ext_vector_type(16)));
typedef unsigned u32x4 __attribute__((ext_vector_type(4)));
typedef unsigned u32x2 __attribute__((ext_vector_type(2)));
typedef __bf16 bf2_t __attribute__((ext_vector_type(2)));
typedef float f32x2 __attribute__((ext_vector_type(2)));
#define LAS __attribute__((address_space(3)))
#define GAS __attribute__((address_space(1)))
#define DI __device__ __forceinline__
typedef __amdgpu_buffer_rsrc_t rsrc_t;

constexpr int S_ = 2048, D_ = 1024, F_ = 3584;
constexpr int HU_ = S_ * 64, HT_ = 2 * HU_, HO_BYTES = 72 * HT_;
constexpr size_t OFF_HO = 0;
constexpr size_t OFF_KV = (size_t)23068672;
constexpr size_t OFF_PX = OFF_KV + (size_t)128 * 65536;
constexpr size_t OFF_FL = OFF_PX + (size_t)256 * 32768;
constexpr int FL_KV = 2, FL_P1 = FL_KV + 1024, FL_E = FL_P1 + 256, FL_RS = FL_E + 1024, FL_N = FL_RS + 1024;
constexpr size_t OFF_CS = OFF_FL + 262144;
constexpr size_t OFF_ST = OFF_CS + 524288;
constexpr int LDS_BYTES = 163840;
constexpr unsigned OOB = 0x40000000u;

#define SBAR() __builtin_amdgcn_sched_barrier(0)
#define BAR() asm volatile("s_barrier" ::: "memory")
#define WAITV(n) asm volatile("s_waitcnt vmcnt(" #n ")" ::: "memory")
#define WAITL0() do { __builtin_amdgcn_s_waitcnt(0xC07F); asm volatile("" ::: "memory"); } while (0)
#define WAITVL_BAR(n) do { __builtin_amdgcn_s_waitcnt(0xC07F); asm volatile("s_waitcnt vmcnt(" #n ")\n\ts_barrier" ::: "memory"); } while (0)
#define WAITV_BAR(n) asm volatile("s_waitcnt vmcnt(" #n ")\n\ts_barrier" ::: "memory")
#define PINS(x) asm volatile("" : "+s"(x))
#define PINV(x) asm volatile("" : "+v"(x))
#define MFMA16(a, b, c) __builtin_amdgcn_mfma_f32_16x16x32_bf16(a, b, c, 0, 0, 0)
#define MFMA32(a, b, c) __builtin_amdgcn_mfma_f32_32x32x16_bf16(a, b, c, 0, 0, 0)
#define RLX_AGENT __ATOMIC_RELAXED, __HIP_MEMORY_SCOPE_AGENT
#define BF8(x) __builtin_bit_cast(bf16x8, x)

#define STAMP(k, i)
#define STAMPC(k, i)

#define STAMPI(k, i)
DI float blo(unsigned v) { return __uint_as_float(v << 16); }
DI float bhi(unsigned v) { return __uint_as_float(v & 0xffff0000u); }
DI unsigned pk2(float lo, float hi) { f32x2 v = {lo, hi}; return __builtin_bit_cast(unsigned, __builtin_convertvector(v, bf2_t)); }
DI float rbf(float f) { return blo(pk2(f, 0.f)); }
DI rsrc_t mkrsrc(const void* p, unsigned bytes) { return __builtin_amdgcn_make_buffer_rsrc((void*)p, (short)0, (int)bytes, 0x00020000); }
DI void dma16(unsigned ldsaddr, unsigned voff, rsrc_t srd, unsigned soff) {
    asm volatile("s_mov_b32 m0, %0\n\ts_nop 0\n\tbuffer_load_dwordx4 %1, %2, %3 offen lds" :: "s"(ldsaddr), "v"(voff), "s"(srd), "s"(soff) : "memory");
}
DI void dma16c(unsigned ldsaddr, unsigned voff, rsrc_t srd, unsigned soff) {
    asm volatile("s_mov_b32 m0, %0\n\ts_nop 0\n\tbuffer_load_dwordx4 %1, %2, %3 offen sc1 lds" :: "s"(ldsaddr), "v"(voff), "s"(srd), "s"(soff) : "memory");
}
DI void dma4(unsigned ldsaddr, unsigned voff, rsrc_t srd, unsigned soff) {
    asm volatile("s_mov_b32 m0, %0\n\ts_nop 0\n\tbuffer_load_dword %1, %2, %3 offen lds" :: "s"(ldsaddr), "v"(voff), "s"(srd), "s"(soff) : "memory");
}
DI void st16c(u32x4 v, rsrc_t srd, unsigned voff, unsigned soff) { __builtin_amdgcn_raw_buffer_store_b128(v, srd, voff + soff, 0, 16); }
DI void put_flag(u64* flags, int unit, u64 a, u64 b, int lane) {
    st16c((u32x4){(unsigned)a, (unsigned)(a >> 32), (unsigned)b, (unsigned)(b >> 32)}, mkrsrc(flags, FL_N * 64), lane < 4 ? (unsigned)(unit * 64 + lane * 16) : OOB, 0);
}
DI bf16x8 lds128(unsigned a) { return *(const LAS bf16x8*)(size_t)a; }
DI s16x4 ldstr(unsigned a) { return __builtin_amdgcn_ds_read_tr16_b64_v4i16((LAS s16x4*)(size_t)a); }
DI bf16x8 cat8(s16x4 lo, s16x4 hi) { return __builtin_shufflevector(lo, hi, 0, 1, 2, 3, 4, 5, 6, 7); }
DI u32x4 pair16(f32x4 a, f32x4 b) {
    unsigned a0 = pk2(a[0], a[1]), a1 = pk2(a[2], a[3]), b0 = pk2(b[0], b[1]), b1 = pk2(b[2], b[3]);
    auto r0 = __builtin_amdgcn_permlane16_swap(a0, b0, false, false);
    auto r1 = __builtin_amdgcn_permlane16_swap(a1, b1, false, false);
    return (u32x4){r0[0], r1[0], r0[1], r1[1]};
}
DI float dot2(unsigned a, unsigned b, float c) { return __builtin_amdgcn_fdot2_f32_bf16(__builtin_bit_cast(bf2_t, a), __builtin_bit_cast(bf2_t, b), c, false); }
DI float addf(float a, float b) { float r; asm("v_add_f32 %0, %1, %2" : "=v"(r) : "v"(a), "v"(b)); return r; }
template <int CTRL> DI float dpp_add(float v) { return v + __int_as_float(__builtin_amdgcn_update_dpp(0, __float_as_int(v), CTRL, 0xf, 0xf, true)); }
DI float half_sum(float s) {
    s = dpp_add<0xB1>(s); s = dpp_add<0x4E>(s); s = dpp_add<0x141>(s); s = dpp_add<0x140>(s);
    auto r = __builtin_amdgcn_permlane16_swap(__float_as_uint(s), __float_as_uint(s), false, false); return __uint_as_float(r[0]) + __uint_as_float(r[1]);
}
template <int V> struct IC { static constexpr int value = V; };
template <int N, int I = 0, class Fn> DI void unroll_for(Fn&& f) { if constexpr (I < N) { f(IC<I>{}); unroll_for<N, I + 1>(f); } }

#define TOUCH_LEAD 8
#define RDSKIP 4
#define K1_PRIO(x)
DI u32x4 pack8(f32x4 a, f32x4 b) { return (u32x4){pk2(a[0], a[1]), pk2(a[2], a[3]), pk2(b[0], b[1]), pk2(b[2], b[3])}; }
struct Ptrs { const u16 *x, *wqkv, *wo, *w1, *b1, *w2, *b2; u16* out; char* ws; };

typedef __attribute__((address_space(4))) const Ptrs* kptr_t;
DI Ptrs args_again() { kptr_t k = (kptr_t)__builtin_amdgcn_kernarg_segment_ptr(); PINS(k); Ptrs r; r.x = k->x; r.wqkv = k->wqkv; r.wo = k->wo; r.w1 = k->w1; r.b1 = k->b1; r.w2 = k->w2; r.b2 = k->b2; r.out = k->out; r.ws = k->ws; return r; }
DI int lane_again() { int l; asm volatile("s_nop 3\n\tv_mbcnt_lo_u32_b32 %0, -1, 0\n\tv_mbcnt_hi_u32_b32 %0, -1, %0" : "=v"(l)); return l; }
DI int bid_again() { int b = blockIdx.x; PINS(b); return b; }
DI int p2_tile(int b) { const int xcd = b & 7, idx = b >> 3; return (2 * (idx & 6) + 2 * (1 - (xcd & 1)) + (idx & 1)) * 8 + 4 * (xcd >> 2) + (idx >> 3); }
DI rsrc_t px_rsrc(char* ws, int b, int other) { return mkrsrc(ws + OFF_PX + (size_t)(p2_tile(b) * 2 + (((b >> 1) & 1) ^ other)) * 32768, 32768); }
DI void px_clear(int w, int lane) {
    const rsrc_t srdS = px_rsrc(args_again().ws, bid_again(), 0);
#pragma unroll
    for (int n = 0; n < 2; ++n) st16c((u32x4){~0u, ~0u, ~0u, ~0u}, srdS, lane * 16, w * 2048 + n * 1024);
}

#define FLAG_EARLY 1
#define L2W 0
#define POLLALL 1
#define EARLY_M 2
#define EMODE 1
#define PSLEEP 1
#define REQ_M 0
#define PROBAR 1
#define G1LATE 1
#define ABL_H 0
#define OUT_AUX 16
#define XT0 0
#define XSPREAD 1
#define FT0 1
#define FSTEP 2
#define FOFF 0
#define FW 1
#define QT0 5
#define QSPREAD 8
#define CODE_TOUCH 3
DI void code_touch(unsigned ldsaddr, int w, int lane) {
    u64 pc; unsigned len;
    asm volatile("s_getpc_b64 %0\n1:\n\ts_mov_b32 %1, .Lfb_end-1b" : "=s"(pc), "=s"(len));
    dma4(ldsaddr, (unsigned)(w * 64 + lane) * 128u, mkrsrc((const void*)pc, len & ~3u), 0);
}

constexpr int K2_AOFF = 81920;
#define K2_EARLY(tau) (35 * h + 7 * ((tau) >> 1) + (((tau) & 1) ? 5 : 1))
DI unsigned k2_wrow(int kt, int ks) {
    const int adj = kt >= 56 ? -112 : ks ? (int)((0x24489122448912ull >> kt) & 1) : -(int)((0x48912244891224ull >> kt) & 1);
    return (unsigned)(2 * kt + ks + adj) * (32 * 2048);
}
DI unsigned k2_voffB(int w4, int lane) { return (unsigned)((4 * w4 + (lane >> 4)) * 2048 + (((lane & 15) ^ (((lane >> 4) | ((w4 >> 1) << 2)) << 1)) << 4)); }
DI void k2_stage_b_tile(unsigned lds0, int tau) {
    const int bid = bid_again(), h = (bid >> 1) & 1, lane = lane_again();
    const rsrc_t srdW2 = mkrsrc(args_again().w2, F_ * D_ * 2); const unsigned soffB0 = (unsigned)((p2_tile(bid) & 7) * 256);
    const unsigned v0 = k2_voffB(0, lane), v2 = k2_voffB(2, lane);
#pragma unroll
    for (int p = 0; p < 16; ++p) dma16(lds0 + tau * 16384 + p * 1024, (p & 2) ? v2 : v0, srdW2, soffB0 + k2_wrow(K2_EARLY(tau), p >> 3) + (unsigned)(4 * (p & 5)) * 2048u);
}
template <int T0, int T1> DI void k2_stage_b(unsigned lds0, int w) {
    const int bid = bid_again(), h = (bid >> 1) & 1, g = w >> 2;
    const rsrc_t srdW2 = mkrsrc(args_again().w2, F_ * D_ * 2); const unsigned voffB = k2_voffB(w & 3, lane_again()), soffB0 = (unsigned)((p2_tile(bid) & 7) * 256);
#pragma unroll
    for (int tau = T0; tau < T1; ++tau)
#pragma unroll
        for (int i = 0; i < 2; ++i) dma16(lds0 + 1024 * (w & 3) + tau * 16384 + (2 * g + i) * 4096, voffB, srdW2, soffB0 + k2_wrow(K2_EARLY(tau), g) + (unsigned)(16 * i) * 2048u);
}

DI const u64* early_flags(char* ws, int b) { const int rt = p2_tile(b) >> 3; return (const u64*)(ws + OFF_FL) + (FL_E + ((rt >> 1) * 16 + 10 * ((b >> 1) & 1)) * 8 + 4 * (rt & 1)) * 8; }
DI int early_word(int lane) { return lane < 24 ? ((lane >> 2) * 8 + (lane & 3)) * 8 : 0; }
DI void early_request(u64& fl, const u64* p) { asm volatile("global_load_dwordx2 %0, %1, %2 sc1" : "=v"(fl) : "v"(early_word(lane_again()) * 8), "s"(p) : "memory"); }

constexpr int K1_ASTG = 32768, K1_BOFF = 2 * K1_ASTG, TAB_ROW = 157696, TAB_COL = TAB_ROW + 2048, TAB_B = TAB_COL + 2048, TAB_ONE = TAB_B + 512;
template <int NF> DI int k1_pair(int y, int wc) { return y == 3 ? 6 + wc : NF == 7 ? (wc ? 8 + 2 * y : 4 - 2 * y) : 4 * y + 2 * wc; }
template <int NF>
DI void k1_mainloop(f32x4 (&acc)[4][NF], unsigned lds0, const Ptrs& P, unsigned soffA0, unsigned soffB0, int w, int lane, int pm, int jt, u64* flags, u64& tag, u64* st) {
    constexpr int PITCH = 64 * NF;
    constexpr int BSTG = 64 * PITCH;
    constexpr int LDB2 = (NF == 7) ? F_ * 2 : 3 * D_ * 2;
    constexpr int DUMMY = K1_BOFF + 3 * BSTG, NS = NF - 2;
    const int wr = w >> 1, wc = w & 1, fr = lane & 15, fq = lane >> 4;
    rsrc_t srdA = mkrsrc(P.x, S_ * D_ * 2), srdB = (NF == 7) ? mkrsrc(P.w1, D_ * F_ * 2) : mkrsrc(P.wqkv, D_ * 3 * D_ * 2);
    PINS(srdA); PINS(srdB);
    const unsigned sbbeg = soffB0, sbend = soffB0 + D_ * LDB2;
    { const int rot = pm < 6 ? 0 : 2 * pm; soffA0 += rot * 128; soffB0 += rot * 64 * LDB2; }
    const unsigned voffA = (unsigned)((128 * (w >> 2) + 8 * (w & 3) + (lane >> 3)) * 2048 + (((lane & 7) ^ (4 * (w & 1) + (lane >> 4))) << 4));
    const unsigned ldsA = lds0 + 1024u * (16 * (w >> 2) + (w & 3));
#define K1_SETA(l_, s_) do { _Pragma("unroll") for (int i = 0; i < 4; ++i) dma16((l_) + i * 4096, voffA, srdA, (s_) + i * (32 * 2048)); } while (0)
    K1_SETA(ldsA, soffA0);
    unsigned voffB;
    {
        const unsigned q = 64 * w + lane, row = (q * (NF == 7 ? 2341u : 2731u)) >> 16, c = q - row * (4 * NF);
        voffB = (NF == 7) ? row * LDB2 + (c << 4) : row * LDB2 + (c >> 3) * 2048 + (((c & 7) ^ (((row >> 1) & 1) << 2)) << 4);
        if (w >= NF) voffB = OOB;
    }
    const unsigned ldsB = (w < NF) ? lds0 + K1_BOFF + 1024u * w : lds0 + DUMMY;
    const unsigned bmul = (w < NF) ? 16u * PITCH : 0u;
#define K1_SETB(l_, s_) do { _Pragma("unroll") for (int i = 0; i < 4; ++i) dma16((l_) + i * bmul, voffB, srdB, (s_) + i * (16 * LDB2)); } while (0)
    K1_SETB(ldsB, soffB0);
    SBAR();
    const unsigned ra0 = lds0 + (64 * wr + fr) * 128 + ((fq ^ (fr >> 1)) << 4), ra1 = ra0 ^ 64u;
    unsigned rb[4];
    {
        const int q = (lane & 15) >> 2, p = lane & 3;
        const unsigned rowp = lds0 + K1_BOFF + (8 * fq + q) * PITCH;
#pragma unroll
        for (int y = 0; y < 3; ++y) { const int f = k1_pair<NF>(y, wc); rb[y] = rowp + 16 * p + ((NF == 7) ? 32 * f : ((32 * f) ^ ((q >> 1) << 6))); }
        rb[3] = rowp + 8 * p + 32 * k1_pair<NF>(3, wc);
    }
    bf16x8 xf[2][4]; s16x4 wl[2][NF], wh[2][NF];
    unsigned ca0 = ra0, ca1 = ra1, cb[4] = {rb[0], rb[1], rb[2], rb[3]};
    f32x4 cs0 = {0.f, 0.f, 0.f, 0.f}, cs1 = {0.f, 0.f, 0.f, 0.f};
#define K1_W(ks, n) cat8(wl[ks][n], wh[ks][n])
#define K1_RW(n_, r) do { constexpr int k_ = ((r) >> 1) & 1; const unsigned b_ = (NF == 7 ? cb[(n_) >> 1] : cb[0] + 128 * ((n_) >> 1)) + 8 * ((n_) & 1) + k_ * (32 * PITCH); \
        if ((r) & 1) wh[k_][n_] = ldstr(b_ + 4 * PITCH); else wl[k_][n_] = ldstr(b_); } while (0)
#define K1_RX(P_, i) xf[P_][i] = lds128((((i) & 1) ? ca1 : ca0) + (2 * (P_) + ((i) >> 1)) * 2048)
#define K1_RD(P_, j) do { if constexpr ((P_) == 1) { if constexpr ((j) < 4) K1_RX(0, j); else if constexpr ((j) < 4 + 4 * NS) K1_RW(((j) >> 2) - 1, j); } \
        else { if constexpr ((j) < 8) K1_RW(NS + ((j) >> 2), j); else if constexpr ((j) < 12) K1_RX(1, (j) - 8); } } while (0)
#define K1_M(P_, WAIT) do { SBAR(); \
        unroll_for<4 * NF>([&](auto J) { constexpr int j = J.value, n = j >> 2, m0 = (j >> 1) & 1, mm = (n & 1) ? 1 - m0 : m0, ks = (j & 1) ^ m0; \
            asm volatile("v_mfma_f32_16x16x32_bf16 %0, %1, %2, %0" : "+v"(acc[2 * (P_) + mm][n]) : "v"(K1_W(ks, n)), "v"(xf[P_][2 * mm + ks])); K1_RD(P_, j); SBAR(); }); \
        WAITL0(); WAIT; BAR(); SBAR(); } while (0)
#define K1_ADDR() do { ca0 = ra0 + oa; ca1 = ra1 + oa; PINV(ca0); PINV(ca1); _Pragma("unroll") for (int i = 0; i < (NF == 7 ? 4 : 1); ++i) { cb[i] = rb[i] + ob; PINV(cb[i]); } } while (0)
#define K1_DA(HOOK) do { \
        dma16(la, voffA, srdA, sa); SBAR(); \
        ca0 = ra0 + oa; ca1 = ra1 + oa; PINV(ca0); PINV(ca1); \
        dma16(la + 4096, voffA, srdA, sa + 32 * 2048); SBAR(); \
        cb[0] = rb[0] + ob; PINV(cb[0]); if (NF == 7) { cb[1] = rb[1] + ob; PINV(cb[1]); } \
        dma16(la + 8192, voffA, srdA, sa + 64 * 2048); SBAR(); \
        if (NF == 7) { cb[2] = rb[2] + ob; cb[3] = rb[3] + ob; PINV(cb[2]); PINV(cb[3]); } \
        lb = (lb == lbend) ? ldsB : lb; sb = (sb == sbend) ? sbbeg : sb; PINS(lb); PINS(sb); \
        dma16(la + 12288, voffA, srdA, sa + 96 * 2048); SBAR(); \
        HOOK; } while (0)
#define K1_DB(HOOK) do { \
        dma16(lb, voffB, srdB, sb); SBAR(); \
        lb += bmul; sb += 16 * LDB2; la ^= K1_ASTG; sa = (sa + 128) & ~2048u; PINS(lb); PINS(sb); PINS(la); PINS(sa); \
        dma16(lb, voffB, srdB, sb); SBAR(); \
        lb += bmul; sb += 16 * LDB2; oa ^= K1_ASTG; ob = (ob == 2 * BSTG) ? 0 : ob + BSTG; PINS(lb); PINS(sb); PINS(oa); PINS(ob); \
        dma16(lb, voffB, srdB, sb); SBAR(); \
        lb += bmul; sb += 16 * LDB2; PINS(lb); PINS(sb); \
        dma16(lb, voffB, srdB, sb); SBAR(); \
        lb += bmul; sb += 16 * LDB2; PINS(lb); PINS(sb); \
        HOOK; } while (0)
#define K1_SYNC_CS() do { u32x4 on_; unsigned oa_; \
        asm volatile("s_cmp_lt_u32 %5, 2\n\ts_cbranch_scc0 9f\n\tv_mov_b32 %3, %6\n\tds_read_b128 %2, %3\n\ts_waitcnt vmcnt(8) lgkmcnt(0)\n\ts_cmp_eq_u32 %4, 0\n\ts_cbranch_scc1 1f\n\ts_cmp_eq_u32 %4, 1\n\ts_cbranch_scc1 2f\n\ts_cmp_eq_u32 %4, 2\n\ts_cbranch_scc1 3f\n\t" \
                     "s_barrier\n\tv_mfma_f32_16x16x32_bf16 %0, %19, %2, %0\n\tv_mfma_f32_16x16x32_bf16 %0, %20, %2, %0\n\ts_branch 8f\n" \
                     "1:\n\ts_barrier\n\tv_mfma_f32_16x16x32_bf16 %0, %7, %2, %0\n\tv_mfma_f32_16x16x32_bf16 %0, %8, %2, %0\n\tv_mfma_f32_16x16x32_bf16 %1, %9, %2, %1\n\tv_mfma_f32_16x16x32_bf16 %1, %10, %2, %1\n\ts_branch 8f\n" \
                     "2:\n\ts_barrier\n\tv_mfma_f32_16x16x32_bf16 %0, %11, %2, %0\n\tv_mfma_f32_16x16x32_bf16 %0, %12, %2, %0\n\tv_mfma_f32_16x16x32_bf16 %1, %13, %2, %1\n\tv_mfma_f32_16x16x32_bf16 %1, %14, %2, %1\n\ts_branch 8f\n" \
                     "3:\n\ts_barrier\n\tv_mfma_f32_16x16x32_bf16 %0, %15, %2, %0\n\tv_mfma_f32_16x16x32_bf16 %0, %16, %2, %0\n\tv_mfma_f32_16x16x32_bf16 %1, %17, %2, %1\n\tv_mfma_f32_16x16x32_bf16 %1, %18, %2, %1\n\ts_branch 8f\n" \
                     "9:\n\ts_waitcnt vmcnt(8)\n\ts_barrier\n8:" \
                     : "+v"(cs0), "+v"(cs1), "=&v"(on_), "=&v"(oa_) : "s"(wr), "s"(t - tb), "s"(lds0 + TAB_ONE), \
                       "v"(K1_W(0, 0)), "v"(K1_W(1, 0)), "v"(K1_W(0, 1)), "v"(K1_W(1, 1)), "v"(K1_W(0, 2)), "v"(K1_W(1, 2)), "v"(K1_W(0, 3)), "v"(K1_W(1, 3)), "v"(K1_W(0, 4)), "v"(K1_W(1, 4)), "v"(K1_W(0, 5)), "v"(K1_W(1, 5)), "v"(K1_W(0, NF - 1)), "v"(K1_W(1, NF - 1)) : "scc", "memory"); } while (0)
#define K1_CSPUB() do { if (__builtin_expect(t - tb == 1, 0)) { \
            PINV(epoch); const unsigned tg_ = (unsigned)epoch + 0x7F4A7C15u; const int l_ = lane_again(), r_ = l_ & 15, q_ = l_ >> 4; \
            const f32x4 c_ = (r_ & 2) ? cs1 : cs0; const bool sg_ = NF == 7 && wr == 3; \
            const unsigned col_ = sg_ ? 16 * k1_pair<NF>(3, wc) + 4 * q_ + 2 * r_ : 16 * k1_pair<NF>(wr, wc) + 8 * q_ + 2 * r_; \
            st16c((u32x4){__float_as_uint((r_ & 1) ? c_[2] : c_[0]), tg_, __float_as_uint((r_ & 1) ? c_[3] : c_[1]), tg_}, mkrsrc(csp + pm * 256, 2048), \
                  (r_ < (sg_ ? 2 : 4) && (NF == 7 || wr < 3)) ? col_ * 8u : OOB, 0); } } while (0)
#pragma unroll
    for (int m = 0; m < 4; ++m)
#pragma unroll
        for (int n = 0; n < NF; ++n) { acc[m][n] = (f32x4){0.f, 0.f, 0.f, 0.f}; PINV(acc[m][n]); }
    asm volatile("" : "+v"(ca0), "+v"(ca1), "+v"(cb[0]), "+v"(cb[1]), "+v"(cb[2]), "+v"(cb[3]));
    *(LAS u32x4*)(size_t)(lds0 + TAB_ONE) = (u32x4){0x3f803f80u, 0x3f803f80u, 0x3f803f80u, 0x3f803f80u};
    SBAR();
    if (PROBAR) BAR();
    K1_SETB(ldsB + 4 * bmul, soffB0 + 64 * LDB2); K1_SETA(ldsA + K1_ASTG, soffA0 + 128);
    const unsigned lbend = ldsB + 12 * bmul;
    unsigned oa = 0, ob = 0;
    unsigned lb = ldsB + 8 * bmul, sb = (soffB0 + 128 * LDB2 == sbend) ? sbbeg : soffB0 + 128 * LDB2, la = ldsA + K1_ASTG, sa = soffA0 + 128;
    STAMPC(0, 11);
    WAITVL_BAR(8);
    STAMPC(0, 12);
    unroll_for<4 + 4 * NS>([&](auto J) { K1_RD(1, J.value); });
    u64 epoch;
    asm volatile("global_load_dwordx2 %0, %1, %2 sc1" : "=v"(epoch) : "v"(0), "s"(flags) : "memory");
    if (NF == 7) dma4(w < 2 ? lds0 + TAB_B + 256 * w : lds0 + DUMMY, w < 2 ? (unsigned)(w * 256 + lane * 4) : OOB, mkrsrc(P.b1, F_ * 2), sbbeg);
    if (G1LATE && w >= 4) { WAITL0(); BAR(); }
    K1_DB();
    WAITL0();
    STAMPC(0, 10);
    if (!G1LATE && w >= 4) BAR();
    const int myrow = (blockIdx.x & 127) * 16 + 2 * w;
    u32x4 xr[4];
#define K1_XROWS() asm volatile("global_load_dwordx4 %0, %4, %5\n\tglobal_load_dwordx4 %1, %4, %5 offset:1024\n\tglobal_load_dwordx4 %2, %4, %5 offset:2048\n\tglobal_load_dwordx4 %3, %4, %5 offset:3072" \
                     : "+v"(xr[0]), "+v"(xr[1]), "+v"(xr[2]), "+v"(xr[3]) : "v"(lane * 16), "s"(P.x + (size_t)myrow * D_) : "memory")
    const int tb = pm < 6 ? 2 * pm : 0;
    u64* const csp = (u64*)(P.ws + OFF_CS) + ((NF == 7 ? 16 : 0) + jt) * 2048;
    asm volatile("" : "=v"(xr[0]), "=v"(xr[1]), "=v"(xr[2]), "=v"(xr[3]));
    const int tx = XT0 + (XSPREAD ? (w & 3) : 0), ts = XT0 + (XSPREAD ? 3 : 0) + 2;
    const int tt = (NF == 7) ? (w < 6 ? FT0 + FSTEP * (w % 3) + (w >= 3 ? FOFF : 0) : FT0 + 3 * FSTEP + (w - 6)) : QT0 + (QSPREAD == 8 ? w : QSPREAD == 4 ? 2 * (w >> 1) + (pm & 1) : pm);
#define K1_TOUCH() do { if (__builtin_expect(t == tt, 0)) { \
            const int l1 = lane_again(); const Ptrs Q = args_again(); const unsigned fb = (unsigned)(pm * 16 + jt); \
            if (NF == 7) dma4(lds0 + DUMMY, (l1 < (w < 3 ? FW * 56 : w < 6 ? FW * 48 : 64)) ? (unsigned)l1 * 128u : OOB, w < 3 ? srdB : w < 6 ? mkrsrc(Q.wqkv, D_ * 3 * D_ * 2) : mkrsrc(Q.wo, D_ * D_ * 2), \
                 w < 3 ? (384 + 128 * w + fb) * (F_ * 2) : w < 6 ? (384 + 128 * (w - 3) + fb) * (3 * D_ * 2) : (fb * 2 + w - 6) * 8192u); \
            else dma4(lds0 + DUMMY, l1 < 56 ? (unsigned)l1 * 128u : OOB, mkrsrc(Q.w2, F_ * D_ * 2), (fb * 8 + w) * 7168u); } } while (0)
#define K1_STATS() do { if (NF == 7) { \
        if (__builtin_expect(t == tx, 0)) { const int lane = lane_again(); K1_XROWS(); } \
        if (__builtin_expect(t == ts, 0)) { \
            asm volatile("" : "+v"(xr[0]), "+v"(xr[1]), "+v"(xr[2]), "+v"(xr[3]), "+v"(epoch)); \
            const int l1 = lane_again(); float s0 = 0.f, s1 = 0.f, q0 = 0.f, q1 = 0.f; \
            _Pragma("unroll") for (int i = 0; i < 8; ++i) { const unsigned a_ = xr[i >> 2][i & 3], b_ = xr[2 + (i >> 2)][i & 3]; \
                s0 = dot2(a_, 0x3f803f80u, s0); q0 = dot2(a_, a_, q0); s1 = dot2(b_, 0x3f803f80u, s1); q1 = dot2(b_, b_, q1); } \
            auto rs_ = __builtin_amdgcn_permlane32_swap(__float_as_uint(s0), __float_as_uint(s1), false, false);     \
            auto rq_ = __builtin_amdgcn_permlane32_swap(__float_as_uint(q0), __float_as_uint(q1), false, false); \
            const float mean = half_sum(__uint_as_float(rs_[0]) + __uint_as_float(rs_[1])) * (1.0f / D_); \
            const float var = __builtin_fmaxf(__builtin_fmaf(-mean, mean, half_sum(__uint_as_float(rq_[0]) + __uint_as_float(rq_[1])) * (1.0f / D_)), 0.f); \
            const float sd = __builtin_amdgcn_sqrtf(rbf(rbf(var) + 1e-5f)); \
            const unsigned ms_ = pk2(mean, sd); auto bo_ = __builtin_amdgcn_permlane32_swap(ms_, ms_, false, false); const u64 tg_ = (u64)((unsigned)epoch + 0x7F4A7C15u) << 32; \
            put_flag(flags, FL_RS + (myrow >> 1), tg_ | bo_[0], tg_ | bo_[1], l1); } } } while (0)
#pragma nounroll
    for (int t = 0; t < 14; ++t) {
        BAR();
        K1_M(0, WAITV(8));
        K1_DA();
        K1_SYNC_CS();
        K1_M(1, );
        K1_DB(K1_CSPUB(); K1_TOUCH(); K1_STATS());
        STAMPI(2, t);
    }
    PINV(epoch);
    tag = (((u64)(unsigned)__builtin_amdgcn_readfirstlane((unsigned)(epoch >> 32)) << 32) | (unsigned)__builtin_amdgcn_readfirstlane((unsigned)epoch)) + 0x9E3779B97F4A7C15ull;
    BAR();
    K1_M(0, WAITV(8));
    K1_ADDR();
    code_touch(lds0 + DUMMY, w, lane_again());
    WAITV_BAR(5);
    K1_M(1, );
    const int lane2 = lane_again();
    const u64* const grp = flags + (FL_RS + pm * 128 + (w & 3) * 32) * 8;
    const unsigned grv = (unsigned)((lane2 >> 1) * 64 + (lane2 & 1) * 8);
    u64 gr;
    asm volatile("s_waitcnt vmcnt(0)\n\tglobal_load_dwordx2 %0, %1, %2 sc1" : "=&v"(gr) : "v"(grv), "s"(grp) : "memory");
    px_clear(w, lane2);
    if (NF == 7) k2_stage_b<0, 2>(lds0, w);
    BAR();
    K1_M(0, );
    if (NF == 7) asm volatile("s_waitcnt vmcnt(6)" : "+v"(gr) :: "memory"); else asm volatile("s_waitcnt vmcnt(2)" : "+v"(gr) :: "memory");
    u32x4 pc[4];
    const u64* const tq = csp + (w & 3) * 64 + 512;
    const unsigned tvo = (unsigned)((lane2 & 31) * 16 + (lane2 >> 5) * 2048);
#define K1_GC(POST) asm volatile("global_load_dwordx4 %0, %4, %6 offset:-4096 sc1\n\tglobal_load_dwordx4 %1, %4, %6 sc1\n\tglobal_load_dwordx4 %2, %5, %6 offset:-4096 sc1\n\tglobal_load_dwordx4 %3, %5, %6 sc1" POST \
        : "=&v"(pc[0]), "=&v"(pc[1]), "=&v"(pc[2]), "=&v"(pc[3]) : "v"(tvo), "v"(tvo + 8192), "s"(tq) : "memory")
    if (w < 4) { K1_GC(""); BAR(); }
    else {
        if (__builtin_expect(!__all((unsigned)(gr >> 32) == (unsigned)tag), 0)) {
            for (unsigned spins = 0; spins < (1u << 20); ++spins) {
                asm volatile("global_load_dwordx2 %0, %1, %2 sc1\n\ts_waitcnt vmcnt(0)" : "=&v"(gr) : "v"(grv), "s"(grp) : "memory");
                if (__all((unsigned)(gr >> 32) == (unsigned)tag)) break;
            }
        }
        if (NF == 7) {
            const unsigned nm = ((unsigned)gr ^ 0x8000u) & 0xffffu;
            *(LAS u32x2*)(size_t)(lds0 + TAB_ROW + ((w - 4) * 64 + lane2) * 8) = (u32x2){nm | (nm << 16), (unsigned)gr ^ 0x8000u};
        } else {
            const float r = __builtin_amdgcn_rcpf(bhi((unsigned)gr));
            *(LAS f32x2*)(size_t)(lds0 + TAB_ROW + ((w - 4) * 64 + lane2) * 8) = (f32x2){r, r * blo((unsigned)gr)};
        }
        asm volatile("" : "=v"(pc[0]), "=v"(pc[1]), "=v"(pc[2]), "=v"(pc[3]));
        BAR();
    }
    K1_M(1, );
    if (w < 4) {
        asm volatile("s_waitcnt vmcnt(0)" : "+v"(pc[0]), "+v"(pc[1]), "+v"(pc[2]), "+v"(pc[3]) :: "memory");
        const bool incol = w * 64 + 2 * (lane2 & 31) < 32 * NF;
#define K1_FRESH() __all(!incol || (min(min(min(pc[0][1], pc[0][3]), min(pc[1][1], pc[1][3])), min(min(pc[2][1], pc[2][3]), min(pc[3][1], pc[3][3]))) == (unsigned)tag && \
                                   max(max(max(pc[0][1], pc[0][3]), max(pc[1][1], pc[1][3])), max(max(pc[2][1], pc[2][3]), max(pc[3][1], pc[3][3]))) == (unsigned)tag))
        if (__builtin_expect(!K1_FRESH(), 0)) {
            for (unsigned spins = 0; spins < (1u << 20); ++spins) { K1_GC("\n\ts_waitcnt vmcnt(0)"); if (K1_FRESH()) break; }
        }
        const float c0 = (__uint_as_float(pc[0][0]) + __uint_as_float(pc[1][0])) + (__uint_as_float(pc[2][0]) + __uint_as_float(pc[3][0]));
        const float c1 = (__uint_as_float(pc[0][2]) + __uint_as_float(pc[1][2])) + (__uint_as_float(pc[2][2]) + __uint_as_float(pc[3][2]));
        auto sw = __builtin_amdgcn_permlane32_swap(__float_as_uint(c0), __float_as_uint(c1), false, false);
        const int cidx = w * 64 + 2 * (lane2 & 31) + (lane2 >> 5); const float c = __uint_as_float(sw[0]) + __uint_as_float(sw[1]);
        if (NF == 7) {
            const unsigned bb = *(const LAS u16*)(size_t)(lds0 + TAB_B + cidx * 2);
            const float c1 = c - bhi(__float_as_uint(c)), c2 = c1 - bhi(__float_as_uint(c1));
            *(LAS u32x2*)(size_t)(lds0 + TAB_COL + cidx * 8) = (u32x2){(__float_as_uint(c) >> 16) | (__float_as_uint(c1) & 0xffff0000u), (__float_as_uint(c2) >> 16) | (bb << 16)};
        } else *(LAS float*)(size_t)(lds0 + TAB_COL + cidx * 4) = c;
        WAITL0(); BAR();
    }
    asm volatile("s_nop 7" ::: "memory"); SBAR();
#undef K1_ADDR
#undef K1_SETA
#undef K1_SETB
#undef K1_W
#undef K1_RW
#undef K1_RX
#undef K1_RD
#undef K1_M
#undef K1_DA
#undef K1_DB
#undef K1_SYNC_CS
#undef K1_CSPUB
#undef K1_XROWS
#undef K1_TOUCH
#undef K1_STATS
#undef K1_GC
#undef K1_FRESH
}

DI f32x2 gelu2(f32x2 a, float k1, float k0, float sd) {
    const f32x2 z = a * __builtin_elementwise_fma(a * a, (f32x2){k1, k1}, (f32x2){k0, k0});
    const f32x2 d = __builtin_elementwise_fma((f32x2){__builtin_amdgcn_exp2f(z[0]), __builtin_amdgcn_exp2f(z[1])}, (f32x2){sd, sd}, (f32x2){sd, sd});
    return a * (f32x2){__builtin_amdgcn_rcpf(d[0]), __builtin_amdgcn_rcpf(d[1])};
}
DI f32x4 gelu4(f32x4 v, float k1, float k0, float sd) {
    const f32x2 lo = gelu2((f32x2){v[0], v[1]}, k1, k0, sd), hi = gelu2((f32x2){v[2], v[3]}, k1, k0, sd);
    return (f32x4){lo[0], lo[1], hi[0], hi[1]};
}
DI f32x4 sp4(float v) { return (f32x4){v, v, v, v}; }
DI f32x4 fma4(f32x4 a, f32x4 b, f32x4 c) { return __builtin_elementwise_fma(a, b, c); }

DI int crow(int r, int hi) { return (r & 3) + 8 * (r >> 2) + 4 * hi; }
constexpr int AT_Q = 0, AT_K = 32768, AT_V = 98304;

template <int MODE> DI void at_mask(f32x16& s, int ql, int hi) {
    if (MODE == 0) return;
#pragma unroll
    for (int r = 0; r < 16; ++r) { const int k = crow(r, hi); const bool keep = (MODE == 1) ? (k <= ql) : (k > ql); s[r] = keep ? s[r] : -INFINITY; }
}
struct AtAddr { unsigned k0, k1, v0, v1; };
template <int MODE> DI float at_max_tile(const AtAddr& a, int kt, bf16x8 q0, bf16x8 q1, float mx, int ql, int hi) {
    f32x16 s = {};
    s = MFMA32(lds128(a.k0 + kt * 2048), q0, s); s = MFMA32(lds128(a.k1 + kt * 2048), q1, s);
    at_mask<MODE>(s, ql, hi);
#pragma unroll
    for (int r = 0; r < 16; r += 2) mx = __builtin_fmaxf(__builtin_fmaxf(mx, s[r]), s[r + 1]);
    return mx;
}
DI float xhalf_max(float m) { auto r = __builtin_amdgcn_permlane32_swap(__float_as_uint(m), __float_as_uint(m), false, false); return __builtin_fmaxf(__uint_as_float(r[0]), __uint_as_float(r[1])); }
DI f32x16 splat16(float v) { f32x16 r; _Pragma("unroll") for (int i = 0; i < 16; ++i) r[i] = v; return r; }
#define AT_LMAX 1.0e18f
struct AtUnit { bf16x8 q0, q1; f32x16 negm, o; f32x4 l; };
DI void at_exp_pack(f32x16 s, u32x4& p0, u32x4& p1) {
#pragma unroll
    for (int r = 0; r < 16; ++r) s[r] = __builtin_amdgcn_exp2f(s[r]);
    p0 = (u32x4){pk2(s[0], s[1]), pk2(s[2], s[3]), pk2(s[4], s[5]), pk2(s[6], s[7])};
    p1 = (u32x4){pk2(s[8], s[9]), pk2(s[10], s[11]), pk2(s[12], s[13]), pk2(s[14], s[15])};
}
DI f32x16 at_scores(const AtUnit& u, bf16x8 k0, bf16x8 k1) { return MFMA32(k1, u.q1, MFMA32(k0, u.q0, u.negm)); }
DI void at_accum(AtUnit& u, f32x16 s, bf16x8 v0, bf16x8 v1, bf16x8 ones) {
    u32x4 p0, p1; at_exp_pack(s, p0, p1);
    u.o = MFMA32(v0, BF8(p0), u.o); u.l = MFMA16(ones, BF8(p0), u.l);
    u.o = MFMA32(v1, BF8(p1), u.o); u.l = MFMA16(ones, BF8(p1), u.l);
}
DI bf16x8 at_v(const AtAddr& a, unsigned off) { return cat8(ldstr(a.v0 + off), ldstr(a.v1 + off)); }
DI void at_single(AtUnit& u, const AtAddr& a, int kt, bf16x8 ones) {
    const unsigned o = kt * 2048;
    const f32x16 s = at_scores(u, lds128(a.k0 + o), lds128(a.k1 + o));
    at_accum(u, s, at_v(a, o), at_v(a, o + 1024), ones);
}
#define AT_EXP4(s_, i) do { s_[i] = __builtin_amdgcn_exp2f(s_[i]); s_[i + 1] = __builtin_amdgcn_exp2f(s_[i + 1]); s_[i + 2] = __builtin_amdgcn_exp2f(s_[i + 2]); s_[i + 3] = __builtin_amdgcn_exp2f(s_[i + 3]); } while (0)
#define AT_G(MF, s_, i, p_) do { MF; AT_EXP4(s_, i); p_[((i) & 7) / 2] = pk2(s_[i], s_[i + 1]); p_[((i) & 7) / 2 + 1] = pk2(s_[i + 2], s_[i + 3]); PINV(p_); SBAR(); } while (0)
DI void at_body(AtUnit& x, AtUnit& y, const AtAddr& a, bf16x8 (&kf)[4], int kt, int ktn, bf16x8 ones) {
    unsigned va = a.v0 + kt * 2048, vb = a.v1 + kt * 2048, ka = a.k0 + ktn * 2048, kb = a.k1 + ktn * 2048;
    asm volatile("" : "+v"(va), "+v"(vb), "+v"(ka), "+v"(kb));
    SBAR();
    bf16x8 vf[4];
#pragma unroll
    for (int i = 0; i < 4; ++i) vf[i] = cat8(ldstr(va + i * 1024), ldstr(vb + i * 1024));
    f32x16 s0 = MFMA32(kf[0], x.q0, x.negm); s0 = MFMA32(kf[1], x.q1, s0);
    f32x16 s1 = MFMA32(kf[0], y.q0, y.negm), s2, s3; PINV(s0); PINV(s1); SBAR();
    u32x4 p0, p1, p2, p3;
    AT_G(s1 = MFMA32(kf[1], y.q1, s1); PINV(s1), s0, 0, p0);
    AT_G(s2 = MFMA32(kf[2], x.q0, x.negm); PINV(s2), s0, 4, p0);
    AT_G(s2 = MFMA32(kf[3], x.q1, s2); PINV(s2), s0, 8, p1);
    AT_G(s3 = MFMA32(kf[2], y.q0, y.negm); PINV(s3), s0, 12, p1);
    AT_G(s3 = MFMA32(kf[3], y.q1, s3); PINV(s3), s1, 0, p2);
    kf[0] = lds128(ka); kf[1] = lds128(kb); kf[2] = lds128(ka + 2048); kf[3] = lds128(kb + 2048);
    AT_G(x.o = MFMA32(vf[0], BF8(p0), x.o); PINV(x.o), s1, 4, p2);
    AT_G(x.o = MFMA32(vf[1], BF8(p1), x.o); PINV(x.o), s1, 8, p3);
    AT_G(x.l = MFMA16(ones, BF8(p0), x.l); x.l = MFMA16(ones, BF8(p1), x.l); PINV(x.l), s1, 12, p3);
    AT_G(y.o = MFMA32(vf[0], BF8(p2), y.o); PINV(y.o), s2, 0, p0);
    AT_G(y.o = MFMA32(vf[1], BF8(p3), y.o); PINV(y.o), s2, 4, p0);
    AT_G(y.l = MFMA16(ones, BF8(p2), y.l); y.l = MFMA16(ones, BF8(p3), y.l); PINV(y.l), s2, 8, p1);
    AT_G(x.o = MFMA32(vf[2], BF8(p0), x.o); PINV(x.o), s2, 12, p1);
    AT_G(x.l = MFMA16(ones, BF8(p0), x.l); PINV(x.l), s3, 0, p2);
    AT_G(x.o = MFMA32(vf[3], BF8(p1), x.o); PINV(x.o), s3, 4, p2);
    AT_G(x.l = MFMA16(ones, BF8(p1), x.l); y.o = MFMA32(vf[2], BF8(p2), y.o); PINV(x.l); PINV(y.o), s3, 8, p3);
    AT_G(y.l = MFMA16(ones, BF8(p2), y.l); PINV(y.l), s3, 12, p3);
    y.o = MFMA32(vf[3], BF8(p3), y.o); y.l = MFMA16(ones, BF8(p3), y.l);
    SBAR();
}
DI void at_kload(bf16x8 (&kf)[4], const AtAddr& a, int kt) { const unsigned ka = a.k0 + kt * 2048, kb = a.k1 + kt * 2048; kf[0] = lds128(ka); kf[1] = lds128(kb); kf[2] = lds128(ka + 2048); kf[3] = lds128(kb + 2048); }
DI void at_masks(u32x4& md0, u32x4& md1, int ql, int hi) {
#pragma unroll
    for (int r = 0; r < 4; ++r) {
        md0[r] = (crow(2 * r, hi) <= ql ? 0xffffu : 0u) | (crow(2 * r + 1, hi) <= ql ? 0xffff0000u : 0u);
        md1[r] = (crow(8 + 2 * r, hi) <= ql ? 0xffffu : 0u) | (crow(9 + 2 * r, hi) <= ql ? 0xffff0000u : 0u);
    }
}
template <bool FAR> DI void at_edges(AtUnit& u, const AtAddr& a, int t, bf16x8 ones, int ql, int hi) {
    const unsigned od = t * 2048, of = od + 16384;
    const bf16x8 kd0 = lds128(a.k0 + od), kd1 = lds128(a.k1 + od), vd0 = at_v(a, od), vd1 = at_v(a, od + 1024);
    bf16x8 kf0, kf1, vf0, vf1;
    if (FAR) { kf0 = lds128(a.k0 + of); kf1 = lds128(a.k1 + of); vf0 = at_v(a, of); vf1 = at_v(a, of + 1024); }
    SBAR();
    f32x16 s = at_scores(u, kd0, kd1);
    if (FAR) {
        const f32x16 sf = at_scores(u, kf0, kf1);
#pragma unroll
        for (int r = 0; r < 16; ++r) s[r] = (crow(r, hi) <= ql) ? s[r] : sf[r];
    } else at_mask<1>(s, ql, hi);
    u32x4 p0, p1; at_exp_pack(s, p0, p1);
    u.l = MFMA16(ones, BF8(p0), u.l); u.l = MFMA16(ones, BF8(p1), u.l);
    if (FAR) {
        u32x4 md0, md1; at_masks(md0, md1, ql, hi);
        const u32x4 d0 = p0 & md0, d1 = p1 & md1;
        u.o = MFMA32(vd0, BF8(d0), u.o); u.o = MFMA32(vd1, BF8(d1), u.o);
        u.o = MFMA32(vf0, BF8(p0 ^ d0), u.o); u.o = MFMA32(vf1, BF8(p1 ^ d1), u.o);
    } else { u.o = MFMA32(vd0, BF8(p0), u.o); u.o = MFMA32(vd1, BF8(p1), u.o); }
}
constexpr u64 diag_lanes(int r) { const int c = (r & 3) + 8 * (r >> 2); return (u64)(0xffffffffu << c) | ((u64)(0xffffffffu << (c + 4)) << 32); }
template <u64 M> DI unsigned pick(unsigned off, unsigned on) { unsigned d; asm("v_cndmask_b32 %0, %1, %2, %3" : "=v"(d) : "v"(off), "v"(on), "s"(M)); return d; }
DI void at_edge_block(AtUnit& x, AtUnit& y, const AtAddr& a, const bf16x8 (&kp)[4], int t, bf16x8 ones) {
    bf16x8 kd[4], vd[4], vp[4];
    at_kload(kd, a, t);
    unsigned va = a.v0 + t * 2048, vb = a.v1 + t * 2048; PINV(va); PINV(vb);
    vp[2] = cat8(ldstr(va + 16384 + 2048), ldstr(vb + 16384 + 2048)); vp[3] = cat8(ldstr(va + 16384 + 3072), ldstr(vb + 16384 + 3072));
    SBAR();
    f32x16 sw = at_scores(x, kp[2], kp[3]), sxf = at_scores(x, kp[0], kp[1]); PINV(sw); PINV(sxf);
    u32x4 md0, md1;
    { unsigned z = 0, lo = 0xffffu, all = ~0u; asm volatile("" : "+v"(z), "+v"(lo), "+v"(all));
      unroll_for<4>([&](auto R) { constexpr int r = R.value;
          md0[r] = pick<diag_lanes(2 * r + 1)>(pick<diag_lanes(2 * r)>(z, lo), all); md1[r] = pick<diag_lanes(9 + 2 * r)>(pick<diag_lanes(8 + 2 * r)>(z, lo), all); }); }
    PINV(md0); PINV(md1); SBAR();
    u32x4 p0, p1; f32x16 sx, sy, syf;
    AT_G(sx = MFMA32(kd[0], x.q0, x.negm); PINV(sx), sw, 0, p0);
    AT_G(sx = MFMA32(kd[1], x.q1, sx); PINV(sx), sw, 4, p0);
    AT_G(syf = MFMA32(kp[2], y.q0, y.negm); PINV(syf), sw, 8, p1);
    AT_G(syf = MFMA32(kp[3], y.q1, syf); PINV(syf), sw, 12, p1);
#pragma unroll
    for (int i = 0; i < 4; ++i) vd[i] = cat8(ldstr(va + i * 1024), ldstr(vb + i * 1024));
    vp[0] = cat8(ldstr(va + 16384), ldstr(vb + 16384)); vp[1] = cat8(ldstr(va + 16384 + 1024), ldstr(vb + 16384 + 1024));
    sy = at_scores(y, kd[2], kd[3]); PINV(sy);
    unroll_for<16>([&](auto R) { constexpr int r = R.value; sx[r] = __uint_as_float(pick<diag_lanes(r)>(__float_as_uint(sxf[r]), __float_as_uint(sx[r]))); });
    PINV(sx); SBAR();
    u32x4 q0, q1;
    AT_G(x.o = MFMA32(vp[2], BF8(p0), x.o); PINV(x.o), sx, 0, q0);
    AT_G(x.o = MFMA32(vp[3], BF8(p1), x.o); PINV(x.o), sx, 4, q0);
    AT_G(x.l = MFMA16(ones, BF8(p0), x.l); x.l = MFMA16(ones, BF8(p1), x.l); PINV(x.l), sx, 8, q1);
    AT_G(x.l = MFMA16(ones, BF8(q0), x.l); PINV(x.l), sx, 12, q1);
    unroll_for<16>([&](auto R) { constexpr int r = R.value; sy[r] = __uint_as_float(pick<diag_lanes(r)>(__float_as_uint(syf[r]), __float_as_uint(sy[r]))); });
    u32x4 d0 = q0 & md0, d1 = q1 & md1, f0 = q0 ^ d0, f1 = q1 ^ d1; PINV(sy); PINV(d0); PINV(d1); PINV(f0); PINV(f1); SBAR();
    AT_G(x.o = MFMA32(vd[0], BF8(d0), x.o); x.l = MFMA16(ones, BF8(q1), x.l); PINV(x.o); PINV(x.l), sy, 0, p0);
    AT_G(x.o = MFMA32(vd[1], BF8(d1), x.o); PINV(x.o), sy, 4, p0);
    AT_G(x.o = MFMA32(vp[0], BF8(f0), x.o); PINV(x.o), sy, 8, p1);
    AT_G(x.o = MFMA32(vp[1], BF8(f1), x.o); PINV(x.o), sy, 12, p1);
    d0 = p0 & md0; d1 = p1 & md1;
    y.o = MFMA32(vd[2], BF8(d0), y.o); y.l = MFMA16(ones, BF8(p0), y.l); y.o = MFMA32(vp[2], BF8(p0 ^ d0), y.o);
    y.o = MFMA32(vd[3], BF8(d1), y.o); y.l = MFMA16(ones, BF8(p1), y.l); y.o = MFMA32(vp[3], BF8(p1 ^ d1), y.o);
}
DI AtAddr at_addr(unsigned lds0, int hd, int lane) {
    const int ql = lane & 31, hi = lane >> 5, blk = (lane >> 4) & 1, q = (lane & 15) >> 2, p = lane & 3, c = 2 * blk + (p >> 1);
    AtAddr a; a.k0 = lds0 + AT_K + hd * 32768 + ql * 64 + ((hi ^ ((ql >> 2) & 3)) << 4); a.k1 = a.k0 ^ 32u;
    const unsigned vb = lds0 + AT_V + hd * 32768 + (4 * hi + q) * 64 + 8 * (p & 1);
    a.v0 = vb + ((c ^ hi) << 4); a.v1 = vb + 512 + ((c ^ (2 + hi)) << 4);
    asm volatile("" : "+v"(a.k0), "+v"(a.k1), "+v"(a.v0), "+v"(a.v1));
    return a;
}
DI void at_init(AtUnit& x, AtUnit& y, const AtAddr& a, unsigned qa, int t, bf16x8 ones) {
    unsigned q0 = qa + t * 2048, q1 = q0 ^ 32u, k0 = a.k0 + t * 2048, k1 = a.k1 + t * 2048, v0 = a.v0 + t * 2048, v1 = a.v1 + t * 2048;
    asm volatile("" : "+v"(q0), "+v"(q1), "+v"(k0), "+v"(k1), "+v"(v0), "+v"(v1));
    const bf16x8 kf0 = lds128(k0), kf1 = lds128(k1);
    y.q0 = lds128(q0 + 2048); y.q1 = lds128(q1 + 2048); x.q0 = lds128(q0); x.q1 = lds128(q1);
    const bf16x8 vf0 = cat8(ldstr(v0), ldstr(v1)), vf1 = cat8(ldstr(v0 + 1024), ldstr(v1 + 1024));
    SBAR();
    const f32x16 z = {};
    f32x16 sy = MFMA32(kf1, y.q1, MFMA32(kf0, y.q0, z)); const f32x16 sx = MFMA32(kf1, x.q1, MFMA32(kf0, x.q0, z));
    const float my = __uint_as_float(__builtin_amdgcn_permlane32_swap(__float_as_uint(sy[0]), __float_as_uint(sy[0]), false, false)[0]);
    const float mx = __uint_as_float(__builtin_amdgcn_permlane32_swap(__float_as_uint(sx[0]), __float_as_uint(sx[0]), false, false)[0]);
    y.negm = splat16(-my); x.negm = splat16(-mx);
    x.o = z; x.l = (f32x4){0.f, 0.f, 0.f, 0.f}; y.o = z; y.l = x.l;
    at_accum(y, sy + y.negm, vf0, vf1, ones);
}
DI void at_check(AtUnit& u, unsigned lds0, int hd, int t, bool has_prev, bf16x8 ones) {
    if (__builtin_expect(__any(!(u.l[0] < AT_LMAX)), 0)) {
        const int lane = lane_again(), ql = lane & 31, hi = lane >> 5; const AtAddr a = at_addr(lds0, hd, lane);
        float mx = at_max_tile<1>(a, t, u.q0, u.q1, -INFINITY, ql, hi);
        for (int kt = 0; kt < t; ++kt) mx = at_max_tile<0>(a, kt, u.q0, u.q1, mx, ql, hi);
        if (has_prev) {
            mx = at_max_tile<2>(a, 8 + t, u.q0, u.q1, mx, ql, hi);
            for (int kt = 9 + t; kt < 16; ++kt) mx = at_max_tile<0>(a, kt, u.q0, u.q1, mx, ql, hi);
        }
        u.negm = splat16(-xhalf_max(mx)); u.o = splat16(0.f); u.l = (f32x4){0.f, 0.f, 0.f, 0.f};
        for (int kt = 0; kt < t; ++kt) at_single(u, a, kt, ones);
        if (has_prev) { at_edges<true>(u, a, t, ones, ql, hi); for (int kt = 9 + t; kt < 16; ++kt) at_single(u, a, kt, ones); }
        else at_edges<false>(u, a, t, ones, ql, hi);
    }
}
DI void at_store(const AtUnit& u, int t, rsrc_t srdO, unsigned voff) {
    const float rl = __builtin_amdgcn_rcpf(u.l[0]);
    u32x4 c[2];
#pragma unroll
    for (int k = 0; k < 2; ++k) {
        unsigned a0 = pk2(u.o[8 * k] * rl, u.o[8 * k + 1] * rl), a1 = pk2(u.o[8 * k + 2] * rl, u.o[8 * k + 3] * rl);
        unsigned b0 = pk2(u.o[8 * k + 4] * rl, u.o[8 * k + 5] * rl), b1 = pk2(u.o[8 * k + 6] * rl, u.o[8 * k + 7] * rl);
        auto r0 = __builtin_amdgcn_permlane32_swap(a0, b0, false, false);
        auto r1 = __builtin_amdgcn_permlane32_swap(a1, b1, false, false);
        c[k] = (u32x4){r0[0], r1[0], r0[1], r1[1]};
    }
#pragma unroll
    for (int i = 0; i < 4; ++i) { auto r = __builtin_amdgcn_permlane16_swap(c[0][i], c[1][i], false, false); c[0][i] = r[0]; c[1][i] = r[1]; }
    st16c(c[0], srdO, voff, (unsigned)(32 * t) * 64u);
    st16c(c[1], srdO, voff, (unsigned)(32 * t + 16) * 64u);
}

#define POLLK 0
DI u64 poll_rmw(const u64* p) { u64 r; asm volatile("global_atomic_or_x2 %0, %1, %2, off sc0 sc1\n\ts_waitcnt vmcnt(0)" : "=&v"(r) : "v"(p), "v"(0ull) : "memory"); return r; }
DI void wait_tags(const u64* p, bool need, u64 tag) {
    for (unsigned spins = 0; spins < (1u << 20); ++spins) {
        const u64 v = POLLK == 0 ? __hip_atomic_load((GAS const u64*)p, RLX_AGENT) : POLLK == 1 ? __hip_atomic_load((GAS const u64*)p, __ATOMIC_RELAXED, __HIP_MEMORY_SCOPE_SYSTEM) : poll_rmw(p);
        if (__all(!need || v == tag)) break;
        __builtin_amdgcn_s_sleep(PSLEEP);
    }
    __builtin_amdgcn_fence(__ATOMIC_ACQUIRE, "wavefront");
}

__global__ __launch_bounds__(512, 2) void fused_block(Ptrs) {
    extern __shared__ __attribute__((aligned(16))) char smem[];
    const unsigned lds0 = (unsigned)(size_t)(LAS char*)smem;
    const int w = __builtin_amdgcn_readfirstlane(threadIdx.x >> 6);
    typedef unsigned u32x16 __attribute__((ext_vector_type(16)));
    u32x16 ka; u64 kws;
    asm volatile("s_load_dwordx16 %0, %2, 0x0\n\ts_load_dwordx2 %1, %2, 0x40" : "=&s"(ka), "=&s"(kws) : "s"(__builtin_amdgcn_kernarg_segment_ptr()));
    if (CODE_TOUCH & 1) code_touch(lds0 + 163584, w, lane_again());
    asm volatile("s_waitcnt lgkmcnt(0)" : "+s"(ka), "+s"(kws));
#define KARG(i) ((const u16*)(((u64)ka[2 * (i) + 1] << 32) | ka[2 * (i)]))
    Ptrs P0; P0.x = KARG(0); P0.wqkv = KARG(1); P0.wo = KARG(2); P0.w1 = KARG(3); P0.b1 = KARG(4); P0.w2 = KARG(5); P0.b2 = KARG(6); P0.out = (u16*)KARG(7); P0.ws = (char*)kws;
    u64* const st = (u64*)(P0.ws + OFF_ST);
    STAMP(0, 0); STAMPC(0, 8);
    u64 fl;
    u64 tag;
    {
        const Ptrs P = P0;
        const int xcd = blockIdx.x & 7, idx = blockIdx.x >> 3;
        u64* const flags = (u64*)(P.ws + OFF_FL);
        const int pm = 2 * (idx & 3) + (xcd & 1), j = 4 * (xcd >> 1) + ((idx >> 2) & 3);
        const int wr = w >> 1, wc = w & 1;
        if (idx >= 16) {
            f32x4 acc[4][7];
            k1_mainloop<7>(acc, lds0, P, (unsigned)(pm * 256 * 2048), (unsigned)(j * 448), w, lane_again(), pm, j, flags, tag, st);
            STAMP(0, 1); STAMPC(0, 9);
            const u64* efp = early_flags(P.ws, bid_again()); PINS(efp);
            const int lane = lane_again(), fr = lane & 15, fq = lane >> 4;
            const rsrc_t srdHO = mkrsrc(args_again().ws + OFF_HO, ABL_H ? 0 : HO_BYTES);
            s16x4 wcor[7], xcor[4]; float k1[4], k0[4], sd[4];
#pragma unroll
            for (int n = 0; n < 7; ++n) wcor[n] = *(const LAS s16x4*)(size_t)(lds0 + TAB_COL + (16 * k1_pair<7>(n >> 1, wc) + (n < 6 ? 8 * (fr >> 2) + 4 * (n & 1) + (fr & 3) : fr)) * 8);
#pragma unroll
            for (int m = 0; m < 4; ++m) {
                const u32x2 t_ = *(const LAS u32x2*)(size_t)(lds0 + TAB_ROW + (64 * wr + 16 * m + fr) * 8);
                xcor[m] = __builtin_bit_cast(s16x4, fq ? (u32x2){0u, 0u} : t_);
                const float c0 = -2.0f * 0.7978845608028654f * 1.4426950408889634f, r = __builtin_amdgcn_rcpf(bhi(t_[1]));
                sd[m] = bhi(t_[1]); k0[m] = c0 * r; k1[m] = (c0 * 0.044715f) * r * r * r;
            }
#define HCOR(m, n) acc[m][n] = __builtin_amdgcn_mfma_f32_16x16x16bf16_1k(wcor[n], xcor[m], acc[m][n], 0, 0, 0)
#define HVAL(m, n) gelu4(acc[m][n], k1[m], k0[m], sd[m])
            HCOR(0, 0); HCOR(0, 1);
            const unsigned vo = (unsigned)(7 * j) * HU_ + (unsigned)(pm * 256 + 64 * wr + fr) * 64u + 16 * (fq ^ ((fr >> 2) & 2));
            const int odd = j & 1;
            STAMP(0, 6);
#pragma unroll
            for (int y = 0; y < 3; ++y) {
                const unsigned uo = (unsigned)((k1_pair<7>(y, wc) >> 1) + (y == 0 ? (wc ? odd - 1 : odd) : 0)) * HU_;
#pragma unroll
                for (int m = 0; m < 4; ++m) {
                    if (4 * y + m < 11) { const int m1 = (m + 1) & 3, y1 = y + (m == 3); HCOR(m1, 2 * y1); HCOR(m1, 2 * y1 + 1); PINV(acc[m1][2 * y1]); PINV(acc[m1][2 * y1 + 1]); }
                    if (y == 0) { HCOR(m, 6); PINV(acc[m][6]); }
                    st16c(pack8(HVAL(m, 2 * y), HVAL(m, 2 * y + 1)), srdHO, vo + uo, 16 * m * 64); SBAR();
                    if (y == 2 && m == REQ_M) { early_request(fl, efp); SBAR(); }
                }
                if (y == 0) {
#pragma unroll
                    for (int m = 0; m < 4; ++m) {
                        const f32x4 hv = HVAL(m, 6); *(LAS u32x2*)(size_t)(lds0 + K2_AOFF + (64 * wr + 16 * m + fr) * 80 + 32 * wc + 8 * fq) = (u32x2){pk2(hv[0], hv[1]), pk2(hv[2], hv[3])};
                        if (m == EARLY_M) { SBAR(); WAITV(0); put_flag(flags, FL_E + (pm * 16 + j) * 8 + w, tag, tag, lane); STAMP(0, 5); SBAR(); }
                    }
                    SBAR();
                }
                if (y == 1) {
                    WAITL0(); BAR();
#pragma unroll
                    for (int i = 0; i < 2; ++i) st16c(*(const LAS u32x4*)(size_t)(lds0 + K2_AOFF + (64 * wr + 32 * wc + 16 * i + fr) * 80 + 16 * fq), srdHO, vo + (unsigned)(4 - 2 * odd) * HU_, (32 * wc + 16 * i) * 64);
                    SBAR();
                }
            }
            STAMP(0, 2);
        } else {
            f32x4 acc[4][6];
            k1_mainloop<6>(acc, lds0, P, (unsigned)(pm * 256 * 2048), (unsigned)(j * 128), w, lane_again(), pm, j, flags, tag, st);
            STAMP(0, 1); STAMPC(0, 9);
            const int lane = lane_again(), fr = lane & 15, fq = lane >> 4;
            {
                f32x2 rr[4];
#pragma unroll
                for (int m = 0; m < 4; ++m) rr[m] = *(const LAS f32x2*)(size_t)(lds0 + TAB_ROW + (64 * wr + 16 * m + fr) * 8);
                const rsrc_t srdX = mkrsrc(P.ws + OFF_KV + (size_t)(pm * 16 + j) * 65536, pm < 7 ? 65536 : 0);
                const unsigned rowoff = (unsigned)((64 * wr + fr) * 64 + ((fq ^ ((fr >> 2) & 3)) << 4));
                f32x4 cc[6];
#pragma unroll
                for (int n = 0; n < 6; ++n) { const unsigned o_ = lds0 + (16 * k1_pair<6>(n >> 1, wc) + 8 * fq + 4 * (n & 1)) * 4; cc[n] = *(const LAS f32x4*)(size_t)(o_ + TAB_COL); }
                SBAR();
#pragma unroll
                for (int y = 1; y < 3; ++y) {
                    const unsigned lb = lds0 + AT_K + (2 * y - 2 + wc) * 32768 + rowoff, go = rowoff + (unsigned)(2 * y - 2 + wc) * 16384u;
#pragma unroll
                    for (int m = 0; m < 4; ++m) {
                        const f32x4 r_ = sp4(rr[m][0]), t_ = sp4(-rr[m][1]);
                        const u32x4 v = pack8(fma4(acc[m][2 * y], r_, cc[2 * y] * t_), fma4(acc[m][2 * y + 1], r_, cc[2 * y + 1] * t_));
                        st16c(v, srdX, go, m * 1024);
                        *(LAS u32x4*)(size_t)(lb + m * 1024) = v;
                    }
                }
                SBAR();
#pragma unroll
                for (int m = 0; m < 4; ++m) {
                    const float sc = 0.17677669529663687f * 1.4426950408889634f;
                    const f32x4 r_ = sp4(rr[m][0] * sc), t_ = sp4(-rr[m][1] * sc);
                    *(LAS u32x4*)(size_t)(lds0 + AT_Q + wc * 16384 + rowoff + m * 1024) = pack8(fma4(acc[m][0], r_, cc[0] * t_), fma4(acc[m][1], r_, cc[1] * t_));
                }
            }
            const int hd = w >> 2, uu = hd ? 7 - w : w, ta = 2 * uu, tb = ta + 1;
            const AtAddr ad = at_addr(lds0, hd, lane);
            const short one = (((lane >> 4) & 1) == ((lane >> 2) & 1)) ? (short)0x3f80 : (short)0;
            bf16x8 ones = {one, one, one, one, one, one, one, one}; PINV(ones);
            if (FLAG_EARLY) { WAITV(0); put_flag(flags, FL_KV + (pm * 16 + j) * 8 + w, tag, tag, lane); }
            WAITL0(); BAR();
            STAMP(0, 6);
            AtUnit ua, ub;
            bf16x8 kf[4];
            at_kload(kf, ad, 0);
            at_init(ua, ub, ad, ad.k0 - AT_K + AT_Q - hd * 16384, ta, ones);
            if (!FLAG_EARLY) { WAITV(0); put_flag(flags, FL_KV + (pm * 16 + j) * 8 + w, tag, tag, lane); }
            STAMP(0, 2);
            if (pm > 0 && uu < 2) {
                const int lane = lane_again();
                wait_tags(flags + (FL_KV + ((pm - 1) * 16 + j) * 8 + (lane & 7)) * 8, true, tag);
                const rsrc_t srdX = mkrsrc(P.ws + OFF_KV + (size_t)((pm - 1) * 16 + j) * 65536, 65536);
                const int rg = w & 3;
#pragma unroll
                for (int q = 0; q < 16; ++q) dma16c(lds0 + AT_K + rg * 32768 + 16384 + q * 1024, lane * 16, srdX, rg * 16384 + q * 1024);
            }
            rsrc_t srdHO = mkrsrc(P.ws + OFF_HO, HO_BYTES); const u64* efp = early_flags(P.ws, bid_again()); PINS(srdHO); PINS(efp);
            for (int i = 0; i < uu; ++i) at_body(ua, ub, ad, kf, 2 * i, 2 * i + 2, ones);
            if (pm > 0) {
                STAMP(0, 3);
                WAITVL_BAR(0);
                STAMP(0, 4);
                if (uu == 3) k2_stage_b_tile(lds0, hd);
                at_kload(kf, ad, 14);
                for (int i = 3; i > uu; --i) at_body(ua, ub, ad, kf, 8 + 2 * i, 6 + 2 * i, ones);
                at_edge_block(ua, ub, ad, kf, ta, ones);
            } else { BAR(); k2_stage_b<0, 2>(lds0, w); const int l2 = lane_again(); const AtAddr a2 = at_addr(lds0, hd, l2); at_edges<false>(ua, a2, ta, ones, l2 & 31, l2 >> 5); at_edges<false>(ub, a2, tb, ones, l2 & 31, l2 >> 5); }
            const int l3 = lane_again();
            const unsigned vo = (unsigned)((112 + 2 * j + hd) * HU_ + (pm * 256 + (l3 & 15)) * 64 + 16 * ((((l3 >> 3) & 2) + (l3 >> 5)) ^ ((l3 >> 2) & 2)));
            at_check(ua, lds0, hd, ta, pm > 0, ones); at_check(ub, lds0, hd, tb, pm > 0, ones);
            early_request(fl, efp);
            at_store(ua, ta, srdHO, vo); at_store(ub, tb, srdHO, vo);
            STAMP(0, 5);
        }
    }
    const Ptrs P = args_again();
    const int lane = lane_again(), fr = lane & 15, fq = lane >> 4;
    const int bid = bid_again(), xcd = bid & 7, idx = bid >> 3;
    u64* const flags = (u64*)(P.ws + OFF_FL);
    const int pm = 2 * (idx & 3) + (xcd & 1), j = 4 * (xcd >> 1) + ((idx >> 2) & 3);
    const int h = (xcd >> 1) & 1, rt = p2_tile(bid) >> 3, ct = p2_tile(bid) & 7;
    const rsrc_t srdHO = mkrsrc(P.ws + OFF_HO, HO_BYTES);
    const int g = w >> 2, w4 = w & 3, wr = w4 >> 1, wc = w4 & 1;
    const rsrc_t srdW2 = mkrsrc(P.w2, F_ * D_ * 2), srdWo = mkrsrc(P.wo, F_ * D_ * 2);
    const unsigned voffA = (unsigned)((w4 ^ (2 * h)) * 1024 + lane * 16), voffB = k2_voffB(w4, lane);
    const unsigned soffA0 = (unsigned)(rt * 128 * 64), soffB0 = (unsigned)(ct * 256);
    const unsigned ldsW = lds0 + 1024u * w4;
#define K2_B0(tau) do { { _Pragma("unroll") for (int i = 0; i < 2; ++i) \
        dma16(ldsW + (tau) * 16384 + (2 * g + i) * 4096, voffB, srdW2, soffB0 + k2_wrow(K2_EARLY(tau), g) + (unsigned)(16 * i) * 2048u); } } while (0)
#define K2_A0(tau) do { const unsigned s_ = soffA0 + (unsigned)(g * HU_ + K2_EARLY(tau) * HT_), l_ = ldsW + K2_AOFF + (tau) * 16384 + g * 8192; \
        dma16c(l_, voffA, srdHO, s_); dma16c(l_ + 4096, voffA, srdHO, s_ + 4096); } while (0)
    const int slot = (idx >= 16) ? j : 16 + j;
    asm volatile("s_cmp_lt_u32 %1, 16\n\ts_cbranch_scc1 1f\n\ts_waitcnt vmcnt(%2)\n1:\n\ts_waitcnt vmcnt(4)" : "+v"(fl) : "s"(idx), "n"(3 - REQ_M) : "scc", "memory");
    if (!__all(fl == tag)) wait_tags(early_flags(P.ws, bid) + early_word(lane), true, tag);
    WAITL0(); BAR();
    STAMP(1, 0); STAMPC(1, 8); STAMPC(1, 13);
    K2_A0(0); K2_A0(1);
    WAITV_BAR(4);
    if (w == 0) { put_flag(flags, FL_P1 + pm * 32 + slot, tag, tag, lane); STAMP(1, 5); }
    K2_B0(2); K2_A0(2); K2_B0(3); K2_A0(3);
    const unsigned ra0 = lds0 + K2_AOFF + (64 * wr + fr) * 64 + ((fq ^ ((fr >> 2) & 2)) << 4);
    unsigned rb[4];
    {
        const int q = (lane & 15) >> 2, p = lane & 3, s = q | ((fq & 1) << 2);
#pragma unroll
        for (int n = 0; n < 4; ++n) rb[n] = lds0 + (8 * fq + q) * 256 + (((4 * wc + n) ^ s) << 5) + 8 * p;
    }
    f32x4 acc[4][4];
    bf16x8 xf[2][2][4]; s16x4 wl[2][2][4], wh[2][2][4];
    unsigned ca0, cb[4];
#define K2_ADDR(buf) do { const unsigned o_ = (buf) * 16384; ca0 = ra0 + o_; _Pragma("unroll") for (int i = 0; i < 4; ++i) cb[i] = rb[i] + o_; \
        asm volatile("" : "+v"(ca0), "+v"(cb[0]), "+v"(cb[1]), "+v"(cb[2]), "+v"(cb[3])); } while (0)
#define K2_RD1(S, r) do { constexpr int f_ = (r) / 3, k_ = f_ >> 2, n_ = f_ & 3; \
        if ((r) % 3 == 0) wl[S][k_][n_] = ldstr(cb[n_] + k_ * 8192); else if ((r) % 3 == 1) wh[S][k_][n_] = ldstr(cb[n_] + k_ * 8192 + 1024); \
        else xf[S][k_][n_] = lds128(ca0 + k_ * 8192 + n_ * 1024); } while (0)
#define K2_MM(S) do { SBAR(); \
        unroll_for<32>([&](auto J) { constexpr int j = J.value, ks = (j & 1) ^ ((j >> 1) & 1), n = j >> 3, m = (n & 1) ? 3 - ((j >> 1) & 3) : ((j >> 1) & 3); \
            asm volatile("v_mfma_f32_16x16x32_bf16 %0, %1, %2, %0" : "+v"(acc[m][n]) : "v"(cat8(wl[S][ks][n], wh[S][ks][n])), "v"(xf[S][ks][m])); \
            if constexpr (j < 24) K2_RD1((S) ^ 1, j); \
            else if constexpr (j < 28) { cb[j - 24] = rb[j - 24] + on; PINV(cb[j - 24]); } \
            else if constexpr (j == 28) { ca0 = ra0 + on; PINV(ca0); } \
            SBAR(); }); } while (0)
#define K2_STEP(S) do { \
        const unsigned l_ = lw, sa_ = sa, sb_ = sb, sc_ = sc; const rsrc_t rb_ = srdB; \
        dma16c(l_ + K2_AOFF, voffA, srdHO, sa_); SBAR(); \
        lw = ldsW + on; on = (on >= 3 * 16384) ? on - 3 * 16384 : on + 2 * 16384; PINS(lw); PINS(on); \
        dma16(l_, voffB, rb_, sb_); SBAR(); \
        kt = 35 * h + (int)__builtin_ctzll(rest | (1ull << 36)); rest &= rest - 1; sa = soffA0 + kt * HT_; PINS(kt); PINS(sa); PINS(rest); \
        dma16c(l_ + K2_AOFF + 4096, voffA, srdHO, sa_ + 4096); SBAR(); \
        sb = soffB0 + k2_wrow(kt, 0); PINS(sb); \
        dma16(l_ + 4096, voffB, rb_, sb_ + 16 * 2048); SBAR(); \
        srdB = (kt >= 56) ? srdWo : srdW2; PINS(srdB); \
        dma16c(l_ + K2_AOFF + 8192, voffA, srdHO, sa_ + HU_); SBAR(); \
        sc = soffB0 + k2_wrow(kt, 1); PINS(sc); \
        dma16(l_ + 8192, voffB, rb_, sc_); \
        dma16c(l_ + K2_AOFF + 12288, voffA, srdHO, sa_ + HU_ + 4096); \
        dma16(l_ + 12288, voffB, rb_, sc_ + 16 * 2048); \
        BAR(); K2_MM(S); WAITVL_BAR(8); SBAR(); } while (0)
    K2_ADDR(g);
#pragma unroll
    for (int m = 0; m < 4; ++m)
#pragma unroll
        for (int n = 0; n < 4; ++n) { acc[m][n] = (f32x4){0.f, 0.f, 0.f, 0.f}; PINV(acc[m][n]); }
    STAMPC(1, 11);
    WAITV_BAR(8);
    STAMPC(1, 12);
    unroll_for<24>([&](auto J) { K2_RD1(0, J.value); });
    const int fw = (FL_P1 + (lane < 32 ? (rt >> 1) * 32 + lane : pm * 32 + (slot ^ 4))) * 8;
    if (w == 4) asm volatile("global_load_dwordx2 %0, %1, %2 sc1" : "+v"(fl) : "v"(fw * 8), "s"(flags) : "memory");
    K2_A0(4); K2_B0(4);
    K2_ADDR(g + 2);
    WAITVL_BAR(8);
    if (g) {
        asm volatile("s_waitcnt vmcnt(4)" : "+v"(fl) :: "memory");
        const bool need = lane >= 32 || (h ? lane >= 10 : lane <= 10);
        if (w == 4 && !__all(!need || fl == tag)) wait_tags(flags + fw, need, tag);
        BAR();
        STAMP(1, 6);
    }
    STAMPC(1, 10);
    int kt = g ? 35 * h + (h ? 2 : 0) : K2_EARLY(5);
    u64 rest = g ? (h ? 0xaaaa52490ull : 0x555524a48ull) : (h ? 0x1555524a48ull : 0xaaaa52494ull);
    unsigned on = (g + 2) * 16384;
    unsigned lw = ldsW + g * 16384, sa = soffA0 + kt * HT_, sb = soffB0 + k2_wrow(kt, 0), sc = soffB0 + k2_wrow(kt, 1);
    rsrc_t srdB = srdW2;
    for (int it = 0; it < 8; ++it) { K2_STEP(0); K2_STEP(1); }
    BAR(); K2_MM(0); WAITVL_BAR(0); SBAR();
    const int lane3 = lane_again();
    const unsigned xa = lds0 + 16384 + w4 * 8192 + lane3 * 16;
    const int fr3 = lane3 & 15, fq3 = lane3 >> 4;
    const int row = rt * 128 + 64 * wr + 32 * (1 - h) + fr3, col = ct * 128 + 64 * wc;
    u32x2 xv[2][4], bv[4];
    if (!g) {
        const rsrc_t srdx = mkrsrc(P.x, S_ * D_ * 2), srdb = mkrsrc(P.b2, D_ * 2);
        const unsigned vb_ = (unsigned)(col + 4 * fq3) * 2u, vx_ = vb_ + (unsigned)row * (D_ * 2);
#pragma unroll
        for (int n = 0; n < 4; ++n) {
            bv[n] = __builtin_amdgcn_raw_buffer_load_b64(srdb, vb_ + 32 * n, 0, 0);
#pragma unroll
            for (int m = 0; m < 2; ++m) xv[m][n] = __builtin_amdgcn_raw_buffer_load_b64(srdx, vx_ + m * (16 * D_ * 2) + 32 * n, 0, 0);
        }
    } else {
#pragma unroll
        for (int n = 0; n < 4; ++n) asm volatile("" : "=v"(bv[n]), "=v"(xv[0][n]), "=v"(xv[1][n]));
    }
    BAR(); K2_MM(1); asm volatile("s_nop 7\n\ts_barrier" ::: "memory"); SBAR();
    if (!g) {
#pragma unroll
        for (int i = 0; i < 8; ++i) *(LAS f32x4*)(size_t)(xa + i * 1024) = acc[i >> 2][i & 3];
        if (bid == 0 && w == 1) {
            for (unsigned spins = 0; spins < (1u << 20); ++spins) {
                bool ok = true;
#pragma unroll
                for (int i = 0; i < 4; ++i) ok = ok & (__hip_atomic_load((GAS const u64*)(flags + (FL_P1 + i * 64 + lane3) * 8), RLX_AGENT) == tag);
                if (__all(ok)) break;
            }
            put_flag(flags, 0, tag, tag, lane3);
        }
        WAITL0(); BAR();
        STAMP(1, 1); STAMPC(1, 9);
#pragma unroll
        for (int i = 0; i < 8; ++i) { const int m = i >> 2, n = i & 3;
            acc[2 + m][n] += (f32x4){blo(xv[m][n][0]) + blo(bv[n][0]), bhi(xv[m][n][0]) + bhi(bv[n][0]), blo(xv[m][n][1]) + blo(bv[n][1]), bhi(xv[m][n][1]) + bhi(bv[n][1])}; }
        const rsrc_t srdR = px_rsrc(P.ws, bid, 1);
        BAR();
#pragma unroll
        for (int i = 0; i < 8; ++i) acc[2 + (i >> 2)][i & 3] += *(const LAS f32x4*)(size_t)(xa + K2_AOFF + i * 1024);
        for (unsigned spins = 0; !POLLALL && spins < (1u << 20); ++spins) {
            if (__builtin_amdgcn_readfirstlane(__builtin_amdgcn_raw_buffer_load_b32(srdR, w4 * 4096 + 4092, 0, 16)) != ~0u) break;
            __builtin_amdgcn_s_sleep(1);
        }
        STAMP(1, 7);
        u32x4 r[4];
        for (unsigned spins = 0; spins < (1u << 20); ++spins) {
            unsigned all = 0;
#pragma unroll
            for (int i = 0; i < 4; ++i) r[i] = __builtin_amdgcn_raw_buffer_load_b128(srdR, lane3 * 16 + w4 * 4096 + i * 1024, 0, 16);
#pragma unroll
            for (int i = 0; i < 4; ++i) all = max(max(all, max(r[i][0], r[i][1])), max(r[i][2], r[i][3]));
            if (__all(all != ~0u)) break;
            asm volatile("" ::: "memory");
        }
        STAMP(1, 3);
#pragma unroll
        for (int i = 0; i < 8; ++i) { const unsigned a_ = r[i >> 1][2 * (i & 1)], b_ = r[i >> 1][2 * (i & 1) + 1]; acc[2 + (i >> 2)][i & 3] += (f32x4){blo(a_), bhi(a_), blo(b_), bhi(b_)}; }
        const rsrc_t srdO = mkrsrc(P.out, S_ * D_ * 2);
        const unsigned vo = (unsigned)(row * D_ + col + 16 * (fq3 & 1) + 8 * (fq3 >> 1)) * 2u;
#pragma unroll
        for (int m = 0; m < 2; ++m) {
            __builtin_amdgcn_raw_buffer_store_b128(pair16(acc[2 + m][0], acc[2 + m][1]), srdO, vo + m * (16 * D_ * 2), 0, OUT_AUX);
            __builtin_amdgcn_raw_buffer_store_b128(pair16(acc[2 + m][2], acc[2 + m][3]), srdO, vo + m * (16 * D_ * 2) + 64, 0, OUT_AUX);
        }
    } else {
        STAMP(1, 1); STAMPC(1, 9);
        const rsrc_t srdS = px_rsrc(P.ws, bid, 0);
        f32x4 pv[8];
#pragma unroll
        for (int i = 0; i < 8; ++i) pv[i] = *(const LAS f32x4*)(size_t)(xa + i * 1024);
        SBAR();
#pragma unroll
        for (int i = 0; i < 8; i += 2)
            st16c(__builtin_elementwise_min(pack8(acc[i >> 2][i & 3] + pv[i], acc[i >> 2][(i & 3) + 1] + pv[i + 1]), (u32x4){~1u, ~1u, ~1u, ~1u}), srdS, lane3 * 16 + w4 * 4096, i * 512);
        STAMP(1, 2);
#pragma unroll
        for (int i = 0; i < 8; ++i) *(LAS f32x4*)(size_t)(xa + K2_AOFF + i * 1024) = acc[2 + (i >> 2)][i & 3];
        WAITL0(); BAR();
    }
    STAMP(1, 4);
    asm volatile(".Lfb_end:");
}

extern "C" void kernel_launch(void* const* d_in, const int* in_sizes, int n_in, void* d_out, int out_size, void* d_ws, size_t ws_size, hipStream_t stream) {
    Ptrs P;
    P.x = (const u16*)d_in[0]; P.wqkv = (const u16*)d_in[1]; P.wo = (const u16*)d_in[2];
    P.w1 = (const u16*)d_in[4]; P.b1 = (const u16*)d_in[5]; P.w2 = (const u16*)d_in[6]; P.b2 = (const u16*)d_in[7];
    P.out = (u16*)d_out; P.ws = (char*)d_ws;
    (void)hipFuncSetAttribute((const void*)fused_block, hipFuncAttributeMaxDynamicSharedMemorySize, LDS_BYTES);
    fused_block<<<256, 512, LDS_BYTES, stream>>>(P);
}
```
